```python
import torch

def _softmax(x, axis=-1):
    return torch.softmax(x, dim=axis)
def _gelu(x):
    return torch.nn.functional.gelu(x, approximate="tanh")
def _silu(x):
    return torch.nn.functional.silu(x)
def _sigmoid(x):
    return torch.sigmoid(x)
def _layernorm(x, axis=-1, eps=1e-5):
    m = x.mean(dim=axis, keepdim=True)
    v = x.var(dim=axis, keepdim=True, unbiased=False)
    return (x - m) / torch.sqrt(v + eps)
def _scatter_add(tgt, idx, src, axis=0):
    return tgt.clone().index_add_(axis, idx, src)
def _segment_sum(src, seg, n):
    out = torch.zeros((n,) + src.shape[1:], dtype=src.dtype)
    return out.index_add_(0, seg, src)
def _pad(x, pw):
    flat = tuple(v for pair in reversed(pw) for v in pair)
    return torch.nn.functional.pad(x, flat)

kernel_name = 'gated_mix__grouped_kv_mqa__ssm_conv_s4__prompt'

def setup_inputs():

    _w = (lambda a: (lambda t: t.to(torch.bfloat16) if t.is_floating_point() else t)(torch.as_tensor(a)))
    def _A_setup():
        B, S, D, H = 1, 2048, 448, 16
        Dh = D // H
        return {
            "x": _w((lambda *s: torch.randn(*s, dtype=torch.bfloat16) * 0.05)(B, S, D)),
            "wq": _w((lambda *s: torch.randn(*s, dtype=torch.bfloat16) * 1.6)(D, H * Dh)),
            "wk": _w((lambda *s: torch.randn(*s, dtype=torch.bfloat16) * 1.6)(D, Dh)),
            "wv": _w((lambda *s: torch.randn(*s, dtype=torch.bfloat16) * 0.05)(D, Dh)),
            "wo": _w((lambda *s: torch.randn(*s, dtype=torch.bfloat16) * 0.05)(H * Dh, D)),
        }
    _A_in = _A_setup()

    def _B_setup():
        B, L, D, N = 1, 2048, 448, 256
        A_re = -(lambda _x: torch.exp(torch.as_tensor(_x)))((lambda *s: torch.randn(*s, dtype=torch.bfloat16) * 3.2)(D, N))
        A_im = torch.pi * (lambda *s: torch.randn(*s, dtype=torch.bfloat16) * 0.05)(D, N)
        B_re = (lambda *s: torch.randn(*s, dtype=torch.bfloat16) * 0.2)(D, N)
        B_im = (lambda *s: torch.randn(*s, dtype=torch.bfloat16) * 0.2)(D, N)
        C_re = (lambda *s: torch.randn(*s, dtype=torch.bfloat16) * 0.2)(D, N)
        C_im = (lambda *s: torch.randn(*s, dtype=torch.bfloat16) * 0.2)(D, N)
        log_dt = (lambda *s: torch.randn(*s, dtype=torch.bfloat16) * 0.05)(D) * 0.5 - 4.0
        Dskip = (lambda *s: torch.randn(*s, dtype=torch.bfloat16) * 0.1)(D)
        return {
            "u": _w((lambda *s: torch.randn(*s, dtype=torch.bfloat16) * 0.05)(B, L, D)),
            "A_re": _w(A_re),
            "A_im": _w(A_im),
            "B_re": _w(B_re),
            "B_im": _w(B_im),
            "C_re": _w(C_re),
            "C_im": _w(C_im),
            "log_dt": _w(log_dt),
            "Dskip": _w(Dskip),
        }
    _B_in = _B_setup()

    return {
        "A_x": _A_in["x"],
        "A_wq": _A_in["wq"],
        "A_wk": _A_in["wk"],
        "A_wv": _A_in["wv"],
        "A_wo": _A_in["wo"],
        "B_u": _B_in["u"],
        "B_A_re": _B_in["A_re"],
        "B_A_im": _B_in["A_im"],
        "B_B_re": _B_in["B_re"],
        "B_B_im": _B_in["B_im"],
        "B_C_re": _B_in["C_re"],
        "B_C_im": _B_in["C_im"],
        "B_log_dt": _B_in["log_dt"],
        "B_Dskip": _B_in["Dskip"],
        "gate_w": _w((lambda *s: torch.randn(*s, dtype=torch.bfloat16) * 0.05)(448) * 16.0),
    }

def reference(A_x, A_wq, A_wk, A_wv, A_wo, B_u, B_A_re, B_A_im, B_B_re, B_B_im, B_C_re, B_C_im, B_log_dt, B_Dskip, gate_w):
    def _A_ref(x, wq, wk, wv, wo):
        B, S, D = x.shape
        H = 16
        Dh = D // H
        q = (lambda x, *d: x.permute(*d))((x @ wq).reshape(B, S, H, Dh), 0, 2, 1, 3)
        k = x @ wk
        v = x @ wv
        scores = torch.einsum("bhqd,bkd->bhqk", q, k) * (Dh ** -0.5)
        p = _softmax(scores, axis=-1)
        o = torch.einsum("bhqk,bkd->bhqd", p, v)
        return (lambda x, *d: x.permute(*d))(o, 0, 2, 1, 3).reshape(B, S, H * Dh) @ wo

    def _B_ref(u, A_re, A_im, B_re, B_im, C_re, C_im, log_dt, Dskip):
        Bsz, L, D = u.shape
        dt = torch.exp(log_dt)[:, None]
        A = A_re + 1j * A_im
        Bc = B_re + 1j * B_im
        Cc = C_re + 1j * C_im
        logdA = dt * A
        dB = dt * Bc
        l = (lambda x, dt: x.to(dt))(torch.arange(L), A_re.dtype)
        P = torch.exp(logdA[:, :, None] * l[None, None, :])
        K = torch.einsum("dn,dnl,dn->dl", Cc, P, dB).real
        L2 = 2 * L
        Kp = _pad(K, ((0, 0), (0, L)))
        up = (lambda x, *d: x.permute(*d))(u, 0, 2, 1)
        up = _pad(up, ((0, 0), (0, 0), (0, L)))
        Kf = (lambda x, axes=(-2,-1): torch.fft.rfft2(x.float(), dim=axes))(Kp[:, None, :], axes=(-2, -1))
        Uf = (lambda x, axes=(-2,-1): torch.fft.rfft2(x.float(), dim=axes))(up[:, :, None, :], axes=(-2, -1))
        Yf = Uf * Kf[None, :, :, :]
        y = (lambda x, s, axes=(-2,-1): torch.fft.irfft2(x, s=s, dim=axes).to(torch.bfloat16))(Yf, (1, L2), axes=(-2, -1))[:, :, 0, :L]
        y = (lambda x, *d: x.permute(*d))(y, 0, 2, 1) + u * Dskip[None, None, :]
        return y

    x = A_x
    g = _sigmoid((x * gate_w).sum(-1, keepdims=True))
    a = _A_ref(x, A_wq, A_wk, A_wv, A_wo)
    b = _B_ref(x, B_A_re, B_A_im, B_B_re, B_B_im, B_C_re, B_C_im, B_log_dt, B_Dskip)
    return g * a + (1 - g) * b
```

```cpp
#include <hip/hip_runtime.h>
#include <hip/hip_bf16.h>
#include <stdint.h>
#define ROUND_L 0
typedef unsigned short u16;
__device__ __forceinline__ float bf2f(u16 b){ return __uint_as_float(((unsigned)b)<<16); }
__device__ __forceinline__ u16 f2bf(float f){ unsigned u=__float_as_uint(f); u += 0x7FFFu + ((u>>16)&1u); return (u16)(u>>16); }
__device__ __forceinline__ float rbf(float f){ return bf2f(f2bf(f)); }
constexpr int S=2048, D=448, H=16, DH=28, N=256;

__global__ void k_proj(const u16* x, const u16* wq, const u16* wk, const u16* wv, const u16* gw, float* q, float* k, float* v, float* g){
  int t=blockIdx.x; __shared__ float xs[D];
  for(int i=threadIdx.x;i<D;i+=blockDim.x) xs[i]=bf2f(x[t*D+i]);
  __syncthreads();
  int c=threadIdx.x;
  if(c<D){ float a=0; for(int i=0;i<D;i++) a+=xs[i]*bf2f(wq[i*D+c]); q[t*D+c]=rbf(a);}
  else if(c<D+DH){ int j=c-D; float a=0; for(int i=0;i<D;i++) a+=xs[i]*bf2f(wk[i*DH+j]); k[t*DH+j]=rbf(a);}
  else if(c<D+2*DH){ int j=c-D-DH; float a=0; for(int i=0;i<D;i++) a+=xs[i]*bf2f(wv[i*DH+j]); v[t*DH+j]=rbf(a);}
  else if(c==D+2*DH){ float a=0; for(int i=0;i<D;i++) a+=rbf(xs[i]*bf2f(gw[i])); a=rbf(a); g[t]=rbf(1.f/(1.f+expf(-a))); }
}
__global__ void k_kgen(const u16* Are,const u16* Aim,const u16* Bre,const u16* Bim,const u16* Cre,const u16* Cim,const u16* ldt,float* K){
  int d=blockIdx.x; __shared__ float ar[N],ai[N],cr[N],ci[N];
  float dt=rbf(expf(bf2f(ldt[d])));
  int n=threadIdx.x;
  { float a=dt*bf2f(Are[d*N+n]), b=dt*bf2f(Aim[d*N+n]); ar[n]=a; ai[n]=b;
    float br=dt*bf2f(Bre[d*N+n]), bi=dt*bf2f(Bim[d*N+n]); float c0=bf2f(Cre[d*N+n]), c1=bf2f(Cim[d*N+n]);
    cr[n]=c0*br-c1*bi; ci[n]=c0*bi+c1*br; }
  __syncthreads();
  for(int l=threadIdx.x;l<S;l+=blockDim.x){
    float lf=(float)l; if(ROUND_L) lf=rbf(lf);
    float acc=0; for(int m=0;m<N;m++){ float e=expf(ar[m]*lf); float s,c; sincosf(ai[m]*lf,&s,&c); acc+=e*(cr[m]*c-ci[m]*s);}
    K[d*S+l]=acc; }
}
__global__ void k_attn(const float* q,const float* k,const float* v,float* o){
  int t=blockIdx.x; int h=threadIdx.x>>4, j=threadIdx.x&15;
  float qq[DH];
  #pragma unroll
  for(int i=0;i<DH;i++) qq[i]=q[t*D+h*DH+i];
  float m=-1e30f,l=0; float acc[DH];
  #pragma unroll
  for(int i=0;i<DH;i++) acc[i]=0;
  const float sc=0.1889822365046136f;
  for(int kk=j;kk<S;kk+=16){ float s=0;
    #pragma unroll
    for(int i=0;i<DH;i++) s+=qq[i]*k[kk*DH+i];
    s*=sc; float mn=fmaxf(m,s); float a=__expf(m-mn), p=__expf(s-mn); l=l*a+p;
    #pragma unroll
    for(int i=0;i<DH;i++) acc[i]=acc[i]*a+p*v[kk*DH+i];
    m=mn; }
  float mm=m; for(int o2=1;o2<16;o2<<=1) mm=fmaxf(mm,__shfl_xor(mm,o2));
  float f=__expf(m-mm); l*=f; for(int o2=1;o2<16;o2<<=1) l+=__shfl_xor(l,o2);
  #pragma unroll
  for(int i=0;i<DH;i++){ float a=acc[i]*f; for(int o2=1;o2<16;o2<<=1) a+=__shfl_xor(a,o2); if(j==0) o[t*D+h*DH+i]=rbf(a/l);}
}
__global__ void k_conv(const u16* x,const float* K,float* y){
  int d=blockIdx.x; __shared__ float xs[S], ks[S];
  for(int i=threadIdx.x;i<S;i+=blockDim.x){ xs[i]=bf2f(x[i*D+d]); ks[i]=K[d*S+i]; }
  __syncthreads();
  for(int t=threadIdx.x;t<S;t+=blockDim.x){ float a=0; for(int s=0;s<=t;s++) a+=ks[t-s]*xs[s]; y[t*D+d]=rbf(a);}
}
__global__ void k_out(const u16* x,const float* o,const u16* wo,const float* y,const float* g,const u16* Dk,u16* out){
  int t=blockIdx.x; __shared__ float os[D];
  for(int i=threadIdx.x;i<D;i+=blockDim.x) os[i]=o[t*D+i];
  __syncthreads();
  int c=threadIdx.x; if(c<D){ float a=0; for(int i=0;i<D;i++) a+=os[i]*bf2f(wo[i*D+c]); a=rbf(a);
    float b=rbf(y[t*D+c]+rbf(bf2f(x[t*D+c])*bf2f(Dk[c]))); float gg=g[t];
    out[t*D+c]=f2bf(rbf(gg*a)+rbf(rbf(1.f-gg)*b)); }
}
extern "C" void kernel_launch(void* const* d_in, const int* in_sizes, int n_in, void* d_out, int out_size, void* d_ws, size_t ws_size, hipStream_t stream){
  const u16* x=(const u16*)d_in[0]; const u16* wq=(const u16*)d_in[1]; const u16* wk=(const u16*)d_in[2]; const u16* wv=(const u16*)d_in[3]; const u16* wo=(const u16*)d_in[4];
  const u16* Are=(const u16*)d_in[6]; const u16* Aim=(const u16*)d_in[7]; const u16* Bre=(const u16*)d_in[8]; const u16* Bim=(const u16*)d_in[9]; const u16* Cre=(const u16*)d_in[10]; const u16* Cim=(const u16*)d_in[11];
  const u16* ldt=(const u16*)d_in[12]; const u16* Dk=(const u16*)d_in[13]; const u16* gw=(const u16*)d_in[14];
  float* ws=(float*)d_ws; float* q=ws; float* k=q+S*D; float* v=k+S*DH; float* g=v+S*DH; float* K=g+S; float* o=K+S*D; float* y=o+S*D;
  k_proj<<<S,512,0,stream>>>(x,wq,wk,wv,gw,q,k,v,g);
  k_kgen<<<D,256,0,stream>>>(Are,Aim,Bre,Bim,Cre,Cim,ldt,K);
  k_attn<<<S,256,0,stream>>>(q,k,v,o);
  k_conv<<<D,256,0,stream>>>(x,K,y);
  k_out<<<S,448,0,stream>>>(x,o,wo,y,g,Dk,(u16*)d_out);
}
```

```cpp
#include <hip/hip_runtime.h>
#include <stdint.h>

#define DI __device__ __forceinline__
#define GAS __attribute__((address_space(1)))
#define LAS __attribute__((address_space(3)))
typedef unsigned u32;
typedef unsigned short u16;
typedef unsigned long long u64;
typedef u32 u32x2 __attribute__((ext_vector_type(2)));
typedef u32 u32x4 __attribute__((ext_vector_type(4)));
typedef float f32x2 __attribute__((ext_vector_type(2)));
typedef float f32x4 __attribute__((ext_vector_type(4)));
typedef float f32x16 __attribute__((ext_vector_type(16)));
typedef __bf16 bf16x2 __attribute__((ext_vector_type(2)));
typedef __bf16 bf16x8 __attribute__((ext_vector_type(8)));
typedef short s16x4 __attribute__((ext_vector_type(4)));
typedef __amdgpu_buffer_rsrc_t rsrc_t;

constexpr u32 WS_FLAGS = 4096, WS_KV = 16384, WS_XT = 0x50000, WS_BT = WS_XT + 224 * 8192, WS_OX = WS_BT + 224 * 8192, WS_END = WS_OX + 256 * 7168;
constexpr u32 L_X = 0, L_W = 28672, L_R1 = 129024, L_MISC = 145664, L_G = L_MISC + 128, L_GW = L_R1 + 14336, L_R2 = 146432, L_TOTAL = 163840;
enum { C_XA = 0, C_PUB, C_WQ, C_KV, C_Q3, C_QX, C_QR, C_BP, C_BR, C_BS, C_XT, C_CX, C_CR, C_WO, C_WR, C_OP0, C_OP1, C_OP2, C_OP3, C_V, C_G1, C_G2, C_OS, C_MX };
constexpr u32 SPIN_CAP = 1u << 20;
constexpr int SC1 = 16;

#define STAMP(i) do { } while (0)
#define STAMP_END() do { } while (0)

template <class T> DI T ld(u32 a) { return *(const LAS T*)(size_t)a; }
template <class T> DI void st(u32 a, T v) { *(LAS T*)(size_t)a = v; }
DI u32x2 ldtr(u32 a) { return __builtin_bit_cast(u32x2, __builtin_amdgcn_ds_read_tr16_b64_v4i16((LAS s16x4*)(size_t)a)); }
DI u32x4 cat(u32x2 a, u32x2 b) { return (u32x4){a[0], a[1], b[0], b[1]}; }
DI u32 pk(float lo, float hi) { return __builtin_bit_cast(u32, __builtin_convertvector((f32x2){lo, hi}, bf16x2)); }
DI bf16x2 asbf2(u32 v) { return __builtin_bit_cast(bf16x2, v); }
DI float bflo(u32 v) { return __uint_as_float(v << 16); }
DI float bfhi(u32 v) { return __uint_as_float(v & 0xffff0000u); }
DI float rbf(float v) { return bflo(pk(v, v)); }
DI u32 nopoison(u32 v) { return v < 0xfffffffeu ? v : 0xfffffffeu; }
DI f32x16 mfma32(u32x4 a, u32x4 b, f32x16 c) { return __builtin_amdgcn_mfma_f32_32x32x16_bf16(__builtin_bit_cast(bf16x8, a), __builtin_bit_cast(bf16x8, b), c, 0, 0, 0); }
DI f32x4 mfma16(u32x4 a, u32x4 b, f32x4 c) { return __builtin_amdgcn_mfma_f32_16x16x32_bf16(__builtin_bit_cast(bf16x8, a), __builtin_bit_cast(bf16x8, b), c, 0, 0, 0); }
DI rsrc_t mkrsrc(const void* p, u32 bytes) { return __builtin_amdgcn_make_buffer_rsrc((void*)p, (short)0, (int)bytes, 0x00020000); }
DI void vm0() { asm volatile("s_waitcnt vmcnt(0)" ::: "memory"); }
#define SIG(c) asm volatile("s_mov_b64 exec, 1\n\tds_add_u32 %0, %1 offset:%2\n\ts_mov_b64 exec, -1" :: "v"(vM), "v"(vOne), "i"(4 * (c)) : "memory")
#define WAITC(c, n, slp) do { u32 v_, s_, k_; asm volatile("s_mov_b32 %2, 0x100000\n1:\n\tds_read_b32 %0, %3 offset:%4\n\ts_waitcnt lgkmcnt(0)\n\tv_readfirstlane_b32 %1, %0\n\t" \
    "s_cmp_ge_u32 %1, %5\n\ts_cbranch_scc1 2f\n\ts_sleep %6\n\ts_sub_u32 %2, %2, 1\n\ts_cbranch_scc0 1b\n2:" \
    : "=&v"(v_), "=&s"(s_), "=&s"(k_) : "v"(vM), "i"(4 * (c)), "s"((u32)(n)), "i"(slp) : "memory", "scc"); } while (0)
#define LANEVARS() int tid_ = tid; asm volatile("" : "+v"(tid_)); const int lane = tid_ & 63, l15 = lane & 15, G = lane >> 4, l31 = lane & 31, h = lane >> 5; \
    const u32 vcol = (lane % 14) * 16 + m * 224
#define SB() __builtin_amdgcn_sched_barrier(0)
#define PRIO(n) __builtin_amdgcn_s_setprio(n)
template <class T> DI T ldv(u32 a) { return *(const volatile LAS T*)(size_t)a; }
DI void dma_run(rsrc_t r, u32 voff, u32 so, u32 ss, u32 send, u32 sw, u32 lo, u32 ls, u32 lw, u32 n) {
    u32 keep, t;
    asm volatile("s_mov_b32 %0, m0\n\ts_nop 2\n1:\n\ts_mov_b32 m0, %3\n\ts_add_u32 %3, %3, %9\n\tbuffer_load_dwordx4 %5, %6, %2 offen lds\n\t"
                 "s_add_u32 %2, %2, %7\n\ts_cmp_ge_u32 %2, %8\n\ts_cselect_b32 %1, %10, 0\n\ts_sub_u32 %2, %2, %1\n\ts_cmp_lg_u32 %1, 0\n\ts_cselect_b32 %1, %11, 0\n\ts_sub_u32 %3, %3, %1\n\t"
                 "s_sub_u32 %4, %4, 1\n\ts_cmp_lg_u32 %4, 0\n\ts_cbranch_scc1 1b\n\ts_mov_b32 m0, %0"
                 : "=&s"(keep), "=&s"(t), "+s"(so), "+s"(lo), "+s"(n) : "v"(voff), "s"(r), "s"(ss), "s"(send), "s"(ls), "s"(sw), "s"(lw) : "memory", "scc");
}
DI f32x2 cmul(f32x2 a, f32x2 b) { f32x2 t = a.xx * b; return __builtin_elementwise_fma(a.yy, (f32x2){-b.y, b.x}, t); }
DI f32x2 zpow(float al, float bl, float p) {
    const float e = __builtin_amdgcn_exp2f(al * p), ph = __builtin_amdgcn_fractf(bl * p);
    return (f32x2){e * __builtin_amdgcn_cosf(ph), e * __builtin_amdgcn_sinf(ph)};
}
DI bool poll_eq(rsrc_t r, u32 off, u32 X) {
    for (u32 i = 0; i < SPIN_CAP; ++i) {
        const u32 y = __builtin_amdgcn_raw_buffer_load_b32(r, off, 0, SC1);
        if (__all(y == X)) return true;
        asm volatile("" ::: "memory");
        __builtin_amdgcn_s_sleep(4);
    }
    return false;
}

#define FRAG8(F, OUT, addr, mask) asm volatile("s_mov_b64 exec, %9\n\tds_read_b128 %0, %8\n\tds_read_b128 %1, %8 offset:32\n\tds_read_b128 %2, %8 offset:64\n\tds_read_b128 %3, %8 offset:96\n\t" \
    "ds_read_b128 %4, %8 offset:128\n\tds_read_b128 %5, %8 offset:160\n\tds_read_b128 %6, %8 offset:192\n\tds_read_b128 %7, %8 offset:224\n\ts_mov_b64 exec, -1" \
    : OUT(F[0]), OUT(F[1]), OUT(F[2]), OUT(F[3]), OUT(F[4]), OUT(F[5]), OUT(F[6]), OUT(F[7]) : "v"(addr), "s"(mask) : "memory")
#define OUT_W(x) "=&v"(x)
#define OUT_RW(x) "+v"(x)

__global__ void __launch_bounds__(512, 2)
fused_mix_kernel(const u16* __restrict__ x, const u16* __restrict__ wq, const u16* __restrict__ wk, const u16* __restrict__ wv, const u16* __restrict__ wo,
                 const u16* __restrict__ Are, const u16* __restrict__ Aim, const u16* __restrict__ Bre, const u16* __restrict__ Bim,
                 const u16* __restrict__ Cre, const u16* __restrict__ Cim, const u16* __restrict__ ldt, const u16* __restrict__ dsk,
                 const u16* __restrict__ gw, u16* __restrict__ out, char* __restrict__ ws) {
    __shared__ __attribute__((aligned(16))) char lds[L_TOTAL];
    const int tid = threadIdx.x, lane = tid & 63;
    int wid = __builtin_amdgcn_readfirstlane(tid >> 6);
    const int b = blockIdx.x, ib = b >> 3, g = (b & 7) + 8 * (ib >> 2), m = ib & 3, rot = ib >> 2;
    u32 stv = 0;
    STAMP(0);
    u32 pf = 0;
    if (wid == 7) {
        u64 pc; u32 len;
        asm volatile("s_getpc_b64 %0\nLpf_here:\n\ts_mov_b32 %1, Lpf_end_a-Lpf_here\n\ts_max_u32 %1, %1, Lpf_end_b-Lpf_here" : "=s"(pc), "=s"(len) :: "scc");
        const u32 o = min((u32)(ib * 2048 + (lane & 15) * 128), len);
        asm volatile("global_load_dword %0, %1, %2" : "=&v"(pf) : "v"(o & ~3u), "s"(pc) : "memory");
    }
    if (tid < 32) st<u32>(L_MISC + 4 * tid, 0u);
    asm volatile("" :: "s"(x), "s"(wq), "s"(wk), "s"(wv), "s"(wo), "s"(Are), "s"(Aim), "s"(Bre), "s"(Bim), "s"(Cre), "s"(Cim), "s"(ldt), "s"(dsk), "s"(gw), "s"(out), "s"(ws));
    asm volatile("s_waitcnt lgkmcnt(0)\n\ts_barrier" :: "s"((u32)(size_t)(LAS char*)lds) : "memory");
    const rsrc_t rws = mkrsrc(ws, WS_END);
    const int l15 = lane & 15, G = lane >> 4, l31 = lane & 31, h = lane >> 5;
    const bool has = b < 224;
    const u32 vM = L_MISC, vOne = 1;
    const u32 vcol = (lane % 14) * 16 + m * 224;
    const rsrc_t rq = mkrsrc(wq, 448 * 896);
    const int s0q = (7 * rot) >> 1;
    u32 pAr = 0, pAi = 0, pBr = 0, pBi = 0, pCr = 0, pCi = 0, pdt = 0, pds = 0;
    if (wid >= 4) {
        const int cg = has ? 2 * b + ((wid - 4) >> 1) : 0;
        const u32 po = (cg * 256 + (wid & 1) * 128 + 2 * lane) * 2;
        pAr = *(const GAS u32*)((const GAS char*)Are + po); pAi = *(const GAS u32*)((const GAS char*)Aim + po); pBr = *(const GAS u32*)((const GAS char*)Bre + po);
        pBi = *(const GAS u32*)((const GAS char*)Bim + po); pCr = *(const GAS u32*)((const GAS char*)Cre + po); pCi = *(const GAS u32*)((const GAS char*)Cim + po);
        pdt = *(const GAS u16*)(ldt + cg); pds = *(const GAS u16*)(dsk + cg);
    } else if (wid >= 2) {
        const u32 vq = (((lane / 14) + 2 * (wid - 2)) & 3) * 3584 + vcol;
        if (lane < 56) {
#pragma unroll
            for (int u = 0; u < 2; ++u) { const int uu = 2 * (wid - 2) + u;
                dma_run(rq, vq, (16 * s0q + uu) * 896, 14336, 448 * 896, 448 * 896, L_W + s0q * 3584 + uu * 896, 3584, 100352, 4); }
        }
    } else {
        const int p0 = 7 * m + wid, par = p0 & 1, s0 = ((((rot * 2 + (m & 1)) * 7) >> 4) * 2) + wid;
        dma_run(mkrsrc(x, 2048 * 896), (32 * g + l15) * 896 + G * 16, (p0 >> 1) * 64 + par * 14336, 64, 896 + par * 14336, 896, L_X + p0 * 1024, 2048, 28672, 14);
        dma_run(mkrsrc(m < 2 ? wk : wv, 448 * 56), (lane >> 1) * 56 + (m & 1) * 32 + (lane & 1) * 16, s0 * 1792, 3584, 25088, 25088, L_R1 + s0 * 1024, 2048, 14336, 7);
        if (wid == 1) dma_run(mkrsrc(gw, 896), lane * 16, 0, 0, ~0u, 0, L_GW, 0, 0, 1);
        else if (lane == 0) {
            u32 keep;
            asm volatile("s_mov_b32 %0, m0\n\ts_mov_b32 m0, %1\n\ts_nop 2\n\tbuffer_load_dword off, %2, 0 sc1 lds\n\ts_mov_b32 m0, %0" : "=&s"(keep) : "s"(L_MISC + 124), "s"(rws) : "memory");
        }
    }
    asm volatile("" : "+s"(wid));

    if (wid < 4) {
        LANEVARS();
        const int a = wid;
        const u32 xb = L_X + (l31 >> 4) * 1024 + h * 256 + l15 * 16;
        u32 X;
        if (a < 2) {
            vm0();
            SIG(C_XA);
            STAMP(1);
            {
                const u32x4 P = {~0u, ~0u, ~0u, ~0u};
                const u32 ox = WS_OX + (g * 4 + m) * 7168 + lane * 16, bt = WS_BT + b * 8192 + lane * 16;
#pragma unroll
                for (int j = 0; j < 4; ++j) {
                    if (j < 3 || a == 0) __builtin_amdgcn_raw_buffer_store_b128(P, rws, ox + j * 2048, a * 1024, SC1);
                    if (has) __builtin_amdgcn_raw_buffer_store_b128(P, rws, bt + j * 2048, a * 1024, SC1);
                }
            }
            WAITC(C_XA, 2, 0);
            STAMP(2);
            {
                const u32 xa = L_X + a * 1024 + lane * 16, wa = L_R1 + (8 * G + (l15 >> 2)) * 32 + (lane & 3) * 8;
                u32x4 Wf[14], Xf[14];
#pragma unroll
                for (int s = 0; s < 14; ++s) { Wf[s] = cat(ldtr(wa + s * 1024), ldtr(wa + s * 1024 + 128)); Xf[s] = ld<u32x4>(xa + s * 2048); }
                SB();
                f32x4 acc = {0.f, 0.f, 0.f, 0.f}, acc2 = acc;
                if (m < 2) {
#pragma unroll
                    for (int s = 0; s < 14; s += 2) { acc = mfma16(Wf[s], Xf[s], acc); acc2 = mfma16(Wf[s + 1], Xf[s + 1], acc2); }
                } else {
#pragma unroll
                    for (int s = 0; s < 14; s += 2) { acc = mfma16(Xf[s], Wf[s], acc); acc2 = mfma16(Xf[s + 1], Wf[s + 1], acc2); }
                }
                acc += acc2;
                u32x2 v = {pk(acc[0], acc[1]), pk(acc[2], acc[3])};
                u32 off;
                if (m < 2) { if (m == 1 && G == 3) v = (u32x2){0x3f803f80u, 0x00003f80u};
                    off = (m & 1) * 1024 + (16 * a + l15 + 32 * (G & 1)) * 16 + 8 * (G >> 1);
                } else { const int d = 16 * (m & 1) + l15; if (d >= 28) v = d == 28 ? (u32x2){0x3f803f80u, 0x3f803f80u} : (u32x2){0u, 0u};
                    off = 2048 + a * 1024 + (d + 32 * (G >> 1)) * 16 + 8 * (G & 1); }
                __builtin_amdgcn_raw_buffer_store_b64(v, rws, WS_KV + g * 4096 + off, 0, SC1);
            }
            {
                const int p = lane & 3, q = l15 >> 2;
                u32x4 v[4];
#pragma unroll
                for (int j = 0; j < 4; ++j) {
                    const int c = 14 * m + 2 * min(a * 4 + j, 6) + (p >> 1);
                    const u32 ad = L_X + (c >> 2) * 2048 + (G >> 1) * 1024 + (c & 3) * 256 + (8 * (G & 1) + q) * 16 + 8 * (p & 1);
                    v[j] = cat(ldtr(ad), ldtr(ad + 64));
                }
                SB();
                const int chn = 112 * m + 64 * a + l15;
                const u32 o = WS_XT + (chn >> 1) * 8192 + g * 128 + (chn & 1) * 64 + G * 16;
#pragma unroll
                for (int j = 0; j < 4; ++j) if (j < 3 || a == 0) __builtin_amdgcn_raw_buffer_store_b128(v[j], rws, o, j * 65536, SC1);
            }
            STAMP(3);
            {
                u32x4 xf[14], gwf[14];
#pragma unroll
                for (int j = 0; j < 14; ++j) { xf[j] = ld<u32x4>(xb + a * 512 + j * 2048); gwf[j] = ld<u32x4>(L_GW + a * 32 + h * 16 + j * 64); }
                X = ld<u32>(L_MISC + 124);
                SIG(C_KV);
                X = __builtin_amdgcn_readfirstlane(X) + 1u;
                float gs = 0.f, gs2 = 0.f;
#pragma unroll
                for (int j = 0; j < 14; ++j) {
                    gs = __builtin_amdgcn_fdot2_f32_bf16(asbf2(xf[j][0]), asbf2(gwf[j][0]), gs, false); gs2 = __builtin_amdgcn_fdot2_f32_bf16(asbf2(xf[j][1]), asbf2(gwf[j][1]), gs2, false);
                    gs = __builtin_amdgcn_fdot2_f32_bf16(asbf2(xf[j][2]), asbf2(gwf[j][2]), gs, false); gs2 = __builtin_amdgcn_fdot2_f32_bf16(asbf2(xf[j][3]), asbf2(gwf[j][3]), gs2, false);
                }
                st<float>(L_G + (a * 64 + lane) * 4, gs + gs2);
            }
            vm0();
            if (lane == 0) { u32 old; asm volatile("ds_add_rtn_u32 %0, %1, %2 offset:%3\n\ts_waitcnt lgkmcnt(0)" : "=&v"(old) : "v"(vM), "v"(vOne), "i"(4 * C_PUB) : "memory");
                if (old == 1) __builtin_amdgcn_raw_buffer_store_b32(X, rws, WS_FLAGS + g * 128 + m * 4, 0, SC1); }
            STAMP(4);
        } else {
            WAITC(C_XA, 2, 0);
            STAMP(2);
            X = __builtin_amdgcn_readfirstlane(ld<u32>(L_MISC + 124)) + 1u;
            const u32 vq = (((lane / 14) + 2 * (a - 2)) & 3) * 3584 + vcol;
            const int s1 = s0q + 4 >= 28 ? s0q - 24 : s0q + 4;
            if (lane < 56) {
#pragma unroll
                for (int u = 0; u < 2; ++u) { const int uu = 2 * (a - 2) + u;
                    dma_run(rq, vq, (16 * s1 + uu) * 896, 14336, 448 * 896, 448 * 896, L_W + s1 * 3584 + uu * 896, 3584, 100352, 24); }
            }
            STAMP(3);
            vm0();
            SIG(C_WQ);
            STAMP(4);
        }
        const rsrc_t rkv = mkrsrc(ws + WS_KV, 64 * 4096);
        u32x4 Q0 = {}, Q1 = {}, Q2 = {}, Q3 = {}, Q4 = {}, Q5 = {}, Q6 = {}, Q7 = {};
        {
            const int q = l15 >> 2;
            const u32 wb = L_W + q * 896 + a * 56 + (G & 1) * 32 + (lane & 3) * 8;
            u32 wa0 = wb + ((2 * h + 2 * (q >> 1)) & 3) * 224, wa1 = wb + ((2 * h + 1 + 2 * (q >> 1)) & 3) * 224;
            for (int pass = 0;; ++pass) {
                asm volatile(
    "s_lshl_b32 s60, %16, 12\n"
    "s_add_u32 s61, s60, 0x1000\n"
    "s_and_b32 s61, s61, 0xffff\n"
    "s_add_u32 s62, s61, 0x1000\n"
    "s_and_b32 s62, s62, 0xffff\n"
    "s_add_u32 s63, s62, 0x1000\n"
    "s_and_b32 s63, s63, 0xffff\n"
    "s_add_u32 s64, s63, 0x1000\n"
    "s_and_b32 s64, s64, 0xffff\n"
    "s_add_u32 s65, s64, 0x1000\n"
    "s_and_b32 s65, s65, 0xffff\n"
    "s_add_u32 s66, s65, 0x1000\n"
    "s_and_b32 s66, s66, 0xffff\n"
    "s_add_u32 s67, s66, 0x1000\n"
    "s_and_b32 s67, s67, 0xffff\n"
    "s_add_u32 s68, s67, 0x1000\n"
    "s_and_b32 s68, s68, 0xffff\n"
    "s_add_u32 s69, s68, 0x1000\n"
    "s_and_b32 s69, s69, 0xffff\n"
    "s_add_u32 s70, s69, 0x1000\n"
    "s_and_b32 s70, s70, 0xffff\n"
    "s_add_u32 s71, s70, 0x1000\n"
    "s_and_b32 s71, s71, 0xffff\n"
    "s_add_u32 s72, s71, 0x1000\n"
    "s_and_b32 s72, s72, 0xffff\n"
    "s_add_u32 s73, s72, 0x1000\n"
    "s_and_b32 s73, s73, 0xffff\n"
    "s_add_u32 s74, s73, 0x1000\n"
    "s_and_b32 s74, s74, 0xffff\n"
    "s_add_u32 s75, s74, 0x1000\n"
    "s_and_b32 s75, s75, 0xffff\n"
    "v_mov_b32 v63, 0xdddd\n"
    "v_mov_b32 v61, 1\n"
    "s_cmp_lg_u32 %17, 0\n"
    "s_cbranch_scc0 Lq_%=\n"
    "v_mov_b32 v192, 0\n"
    "v_mov_b32 v193, 0\n"
    "v_mov_b32 v194, 0\n"
    "v_mov_b32 v195, 0\n"
    "v_mov_b32 v196, 0\n"
    "v_mov_b32 v197, 0\n"
    "v_mov_b32 v198, 0\n"
    "v_mov_b32 v199, 0\n"
    "v_mov_b32 v200, 0\n"
    "v_mov_b32 v201, 0\n"
    "v_mov_b32 v202, 0\n"
    "v_mov_b32 v203, 0\n"
    "v_mov_b32 v204, 0\n"
    "v_mov_b32 v205, 0\n"
    "v_mov_b32 v206, 0\n"
    "v_mov_b32 v207, 0\n"
    "v_mov_b32 v208, 0\n"
    "v_mov_b32 v209, 0\n"
    "v_mov_b32 v210, 0\n"
    "v_mov_b32 v211, 0\n"
    "v_mov_b32 v212, 0\n"
    "v_mov_b32 v213, 0\n"
    "v_mov_b32 v214, 0\n"
    "v_mov_b32 v215, 0\n"
    "v_mov_b32 v216, 0\n"
    "v_mov_b32 v217, 0\n"
    "v_mov_b32 v218, 0\n"
    "v_mov_b32 v219, 0\n"
    "v_mov_b32 v220, 0\n"
    "v_mov_b32 v221, 0\n"
    "v_mov_b32 v222, 0\n"
    "v_mov_b32 v223, 0\n"
    "v_mov_b32 v224, 0\n"
    "v_mov_b32 v225, 0\n"
    "v_mov_b32 v226, 0\n"
    "v_mov_b32 v227, 0\n"
    "v_mov_b32 v228, 0\n"
    "v_mov_b32 v229, 0\n"
    "v_mov_b32 v230, 0\n"
    "v_mov_b32 v231, 0\n"
    "v_mov_b32 v232, 0\n"
    "v_mov_b32 v233, 0\n"
    "v_mov_b32 v234, 0\n"
    "v_mov_b32 v235, 0\n"
    "v_mov_b32 v236, 0\n"
    "v_mov_b32 v237, 0\n"
    "v_mov_b32 v238, 0\n"
    "v_mov_b32 v239, 0\n"
    "v_mov_b32 v240, 0\n"
    "v_mov_b32 v241, 0\n"
    "v_mov_b32 v242, 0\n"
    "v_mov_b32 v243, 0\n"
    "v_mov_b32 v244, 0\n"
    "v_mov_b32 v245, 0\n"
    "v_mov_b32 v246, 0\n"
    "v_mov_b32 v247, 0\n"
    "v_mov_b32 v248, 0\n"
    "v_mov_b32 v249, 0\n"
    "v_mov_b32 v250, 0\n"
    "v_mov_b32 v251, 0\n"
    "v_mov_b32 v252, 0\n"
    "v_mov_b32 v253, 0\n"
    "v_mov_b32 v254, 0\n"
    "v_mov_b32 v255, 0\n"
    "buffer_load_dwordx4 v[64:67], %13, %11, s60 offen offset:0 sc1\n"
    "buffer_load_dwordx4 v[68:71], %13, %11, s60 offen offset:1024 sc1\n"
    "buffer_load_dwordx4 v[72:75], %13, %11, s60 offen offset:2048 sc1\n"
    "buffer_load_dwordx4 v[76:79], %13, %11, s60 offen offset:3072 sc1\n"
    "buffer_load_dwordx4 v[80:83], %13, %11, s61 offen offset:0 sc1\n"
    "buffer_load_dwordx4 v[84:87], %13, %11, s61 offen offset:1024 sc1\n"
    "buffer_load_dwordx4 v[88:91], %13, %11, s61 offen offset:2048 sc1\n"
    "buffer_load_dwordx4 v[92:95], %13, %11, s61 offen offset:3072 sc1\n"
    "buffer_load_dwordx4 v[96:99], %13, %11, s62 offen offset:0 sc1\n"
    "buffer_load_dwordx4 v[100:103], %13, %11, s62 offen offset:1024 sc1\n"
    "buffer_load_dwordx4 v[104:107], %13, %11, s62 offen offset:2048 sc1\n"
    "buffer_load_dwordx4 v[108:111], %13, %11, s62 offen offset:3072 sc1\n"
    "buffer_load_dwordx4 v[112:115], %13, %11, s63 offen offset:0 sc1\n"
    "buffer_load_dwordx4 v[116:119], %13, %11, s63 offen offset:1024 sc1\n"
    "buffer_load_dwordx4 v[120:123], %13, %11, s63 offen offset:2048 sc1\n"
    "buffer_load_dwordx4 v[124:127], %13, %11, s63 offen offset:3072 sc1\n"
    "buffer_load_dwordx4 v[128:131], %13, %11, s64 offen offset:0 sc1\n"
    "buffer_load_dwordx4 v[132:135], %13, %11, s64 offen offset:1024 sc1\n"
    "buffer_load_dwordx4 v[136:139], %13, %11, s64 offen offset:2048 sc1\n"
    "buffer_load_dwordx4 v[140:143], %13, %11, s64 offen offset:3072 sc1\n"
    "s_branch Lnoq_%=\n"
    "Lq_%=:\n"
    "s_mov_b32 s58, 0x100000\n"
    "Lwq_%=:\n"
    "ds_read_b32 v62, %21 offset:8\n"
    "s_waitcnt lgkmcnt(0)\n"
    "v_readfirstlane_b32 s59, v62\n"
    "s_cmp_ge_u32 s59, 2\n"
    "s_cbranch_scc1 Lwqx_%=\n"
    "s_sub_u32 s58, s58, 1\n"
    "s_cbranch_scc0 Lwq_%=\n"
    "Lwqx_%=:\n"
    "buffer_load_dword v60, %19, %12, 0 offen sc1\n"
    "ds_read_b64_tr_b16 v[144:145], %9 offset:0\n"
    "ds_read_b64_tr_b16 v[146:147], %10 offset:0\n"
    "ds_read_b128 v[148:151], %18 offset:0\n"
    "ds_read_b64_tr_b16 v[152:153], %9 offset:3584\n"
    "ds_read_b64_tr_b16 v[154:155], %10 offset:3584\n"
    "ds_read_b128 v[156:159], %18 offset:512\n"
    "ds_read_b64_tr_b16 v[160:161], %9 offset:7168\n"
    "ds_read_b64_tr_b16 v[162:163], %10 offset:7168\n"
    "ds_read_b128 v[164:167], %18 offset:2048\n"
    "ds_read_b64_tr_b16 v[168:169], %9 offset:10752\n"
    "ds_read_b64_tr_b16 v[170:171], %10 offset:10752\n"
    "ds_read_b128 v[172:175], %18 offset:2560\n"
    "s_mov_b64 exec, 1\n"
    "ds_add_u32 %21, v61 offset:16\n"
    "s_mov_b64 exec, -1\n"
    "s_mov_b32 s57, 0\n"
    "s_waitcnt lgkmcnt(9)\n"
    "v_mfma_f32_32x32x16_bf16 v[176:191], v[144:147], v[148:151], 0\n"
    "ds_read_b64_tr_b16 v[144:145], %9 offset:14336\n"
    "ds_read_b64_tr_b16 v[146:147], %10 offset:14336\n"
    "ds_read_b128 v[148:151], %18 offset:4096\n"
    "v_mov_b32 v255, 0\n"
    "v_mov_b32 v254, 0\n"
    "v_mov_b32 v253, 0\n"
    "s_waitcnt lgkmcnt(9)\n"
    "v_mfma_f32_32x32x16_bf16 v[176:191], v[152:155], v[156:159], v[176:191]\n"
    "ds_read_b64_tr_b16 v[152:153], %9 offset:17920\n"
    "ds_read_b64_tr_b16 v[154:155], %10 offset:17920\n"
    "ds_read_b128 v[156:159], %18 offset:4608\n"
    "v_mov_b32 v252, 0\n"
    "v_mov_b32 v251, 0\n"
    "v_mov_b32 v250, 0\n"
    "s_waitcnt lgkmcnt(9)\n"
    "v_mfma_f32_32x32x16_bf16 v[176:191], v[160:163], v[164:167], v[176:191]\n"
    "ds_read_b64_tr_b16 v[160:161], %9 offset:21504\n"
    "ds_read_b64_tr_b16 v[162:163], %10 offset:21504\n"
    "ds_read_b128 v[164:167], %18 offset:6144\n"
    "v_mov_b32 v249, 0\n"
    "v_mov_b32 v248, 0\n"
    "v_mov_b32 v247, 0\n"
    "s_waitcnt lgkmcnt(9)\n"
    "v_mfma_f32_32x32x16_bf16 v[176:191], v[168:171], v[172:175], v[176:191]\n"
    "ds_read_b64_tr_b16 v[168:169], %9 offset:25088\n"
    "ds_read_b64_tr_b16 v[170:171], %10 offset:25088\n"
    "ds_read_b128 v[172:175], %18 offset:6656\n"
    "v_mov_b32 v246, 0\n"
    "v_mov_b32 v245, 0\n"
    "v_mov_b32 v244, 0\n"
    "s_waitcnt lgkmcnt(9)\n"
    "v_mfma_f32_32x32x16_bf16 v[176:191], v[144:147], v[148:151], v[176:191]\n"
    "ds_read_b64_tr_b16 v[144:145], %9 offset:28672\n"
    "ds_read_b64_tr_b16 v[146:147], %10 offset:28672\n"
    "ds_read_b128 v[148:151], %18 offset:8192\n"
    "v_mov_b32 v243, 0\n"
    "v_mov_b32 v242, 0\n"
    "v_mov_b32 v241, 0\n"
    "s_waitcnt lgkmcnt(9)\n"
    "v_mfma_f32_32x32x16_bf16 v[176:191], v[152:155], v[156:159], v[176:191]\n"
    "ds_read_b64_tr_b16 v[152:153], %9 offset:32256\n"
    "ds_read_b64_tr_b16 v[154:155], %10 offset:32256\n"
    "ds_read_b128 v[156:159], %18 offset:8704\n"
    "v_mov_b32 v240, 0\n"
    "v_mov_b32 v239, 0\n"
    "v_mov_b32 v238, 0\n"
    "s_waitcnt lgkmcnt(9)\n"
    "v_mfma_f32_32x32x16_bf16 v[176:191], v[160:163], v[164:167], v[176:191]\n"
    "ds_read_b64_tr_b16 v[160:161], %9 offset:35840\n"
    "ds_read_b64_tr_b16 v[162:163], %10 offset:35840\n"
    "ds_read_b128 v[164:167], %18 offset:10240\n"
    "v_mov_b32 v237, 0\n"
    "v_mov_b32 v236, 0\n"
    "v_mov_b32 v235, 0\n"
    "s_waitcnt lgkmcnt(9)\n"
    "v_mfma_f32_32x32x16_bf16 v[176:191], v[168:171], v[172:175], v[176:191]\n"
    "ds_read_b64_tr_b16 v[168:169], %9 offset:39424\n"
    "ds_read_b64_tr_b16 v[170:171], %10 offset:39424\n"
    "ds_read_b128 v[172:175], %18 offset:10752\n"
    "v_mov_b32 v234, 0\n"
    "v_mov_b32 v233, 0\n"
    "v_mov_b32 v232, 0\n"
    "s_waitcnt lgkmcnt(9)\n"
    "v_mfma_f32_32x32x16_bf16 v[176:191], v[144:147], v[148:151], v[176:191]\n"
    "ds_read_b64_tr_b16 v[144:145], %9 offset:43008\n"
    "ds_read_b64_tr_b16 v[146:147], %10 offset:43008\n"
    "ds_read_b128 v[148:151], %18 offset:12288\n"
    "v_mov_b32 v231, 0\n"
    "v_mov_b32 v230, 0\n"
    "v_mov_b32 v229, 0\n"
    "s_waitcnt lgkmcnt(9)\n"
    "v_mfma_f32_32x32x16_bf16 v[176:191], v[152:155], v[156:159], v[176:191]\n"
    "ds_read_b64_tr_b16 v[152:153], %9 offset:46592\n"
    "ds_read_b64_tr_b16 v[154:155], %10 offset:46592\n"
    "ds_read_b128 v[156:159], %18 offset:12800\n"
    "v_mov_b32 v228, 0\n"
    "v_mov_b32 v227, 0\n"
    "v_mov_b32 v226, 0\n"
    "s_waitcnt lgkmcnt(9)\n"
    "v_mfma_f32_32x32x16_bf16 v[176:191], v[160:163], v[164:167], v[176:191]\n"
    "ds_read_b64_tr_b16 v[160:161], %9 offset:50176\n"
    "ds_read_b64_tr_b16 v[162:163], %10 offset:50176\n"
    "ds_read_b128 v[164:167], %18 offset:14336\n"
    "v_mov_b32 v225, 0\n"
    "v_mov_b32 v224, 0\n"
    "v_mov_b32 v223, 0\n"
    "s_waitcnt lgkmcnt(9)\n"
    "v_mfma_f32_32x32x16_bf16 v[176:191], v[168:171], v[172:175], v[176:191]\n"
    "ds_read_b64_tr_b16 v[168:169], %9 offset:53760\n"
    "ds_read_b64_tr_b16 v[170:171], %10 offset:53760\n"
    "ds_read_b128 v[172:175], %18 offset:14848\n"
    "v_mov_b32 v222, 0\n"
    "v_mov_b32 v221, 0\n"
    "v_mov_b32 v220, 0\n"
    "s_waitcnt lgkmcnt(9)\n"
    "v_mfma_f32_32x32x16_bf16 v[176:191], v[144:147], v[148:151], v[176:191]\n"
    "ds_read_b64_tr_b16 v[144:145], %9 offset:57344\n"
    "ds_read_b64_tr_b16 v[146:147], %10 offset:57344\n"
    "ds_read_b128 v[148:151], %18 offset:16384\n"
    "v_mov_b32 v219, 0\n"
    "v_mov_b32 v218, 0\n"
    "v_mov_b32 v217, 0\n"
    "s_waitcnt lgkmcnt(9)\n"
    "v_mfma_f32_32x32x16_bf16 v[176:191], v[152:155], v[156:159], v[176:191]\n"
    "ds_read_b64_tr_b16 v[152:153], %9 offset:60928\n"
    "ds_read_b64_tr_b16 v[154:155], %10 offset:60928\n"
    "ds_read_b128 v[156:159], %18 offset:16896\n"
    "v_mov_b32 v216, 0\n"
    "v_mov_b32 v215, 0\n"
    "v_mov_b32 v214, 0\n"
    "s_waitcnt lgkmcnt(9)\n"
    "v_mfma_f32_32x32x16_bf16 v[176:191], v[160:163], v[164:167], v[176:191]\n"
    "v_add_u32 %9, 64512, %9\n"
    "v_add_u32 %10, 64512, %10\n"
    "ds_read_b64_tr_b16 v[160:161], %9 offset:0\n"
    "ds_read_b64_tr_b16 v[162:163], %10 offset:0\n"
    "ds_read_b128 v[164:167], %18 offset:18432\n"
    "v_mov_b32 v213, 0\n"
    "v_mov_b32 v212, 0\n"
    "v_mov_b32 v211, 0\n"
    "s_waitcnt vmcnt(0)\n"
    "v_cmp_ne_u32 vcc, %20, v60\n"
    "s_nop 0\n"
    "s_cmp_eq_u64 vcc, 0\n"
    "s_cbranch_scc0 Lck1_%=\n"
    "buffer_load_dwordx4 v[64:67], %13, %11, s60 offen offset:0 sc1\n"
    "buffer_load_dwordx4 v[68:71], %13, %11, s60 offen offset:1024 sc1\n"
    "buffer_load_dwordx4 v[72:75], %13, %11, s60 offen offset:2048 sc1\n"
    "buffer_load_dwordx4 v[76:79], %13, %11, s60 offen offset:3072 sc1\n"
    "buffer_load_dwordx4 v[80:83], %13, %11, s61 offen offset:0 sc1\n"
    "buffer_load_dwordx4 v[84:87], %13, %11, s61 offen offset:1024 sc1\n"
    "buffer_load_dwordx4 v[88:91], %13, %11, s61 offen offset:2048 sc1\n"
    "buffer_load_dwordx4 v[92:95], %13, %11, s61 offen offset:3072 sc1\n"
    "buffer_load_dwordx4 v[96:99], %13, %11, s62 offen offset:0 sc1\n"
    "buffer_load_dwordx4 v[100:103], %13, %11, s62 offen offset:1024 sc1\n"
    "buffer_load_dwordx4 v[104:107], %13, %11, s62 offen offset:2048 sc1\n"
    "buffer_load_dwordx4 v[108:111], %13, %11, s62 offen offset:3072 sc1\n"
    "buffer_load_dwordx4 v[112:115], %13, %11, s63 offen offset:0 sc1\n"
    "buffer_load_dwordx4 v[116:119], %13, %11, s63 offen offset:1024 sc1\n"
    "buffer_load_dwordx4 v[120:123], %13, %11, s63 offen offset:2048 sc1\n"
    "buffer_load_dwordx4 v[124:127], %13, %11, s63 offen offset:3072 sc1\n"
    "buffer_load_dwordx4 v[128:131], %13, %11, s64 offen offset:0 sc1\n"
    "buffer_load_dwordx4 v[132:135], %13, %11, s64 offen offset:1024 sc1\n"
    "buffer_load_dwordx4 v[136:139], %13, %11, s64 offen offset:2048 sc1\n"
    "buffer_load_dwordx4 v[140:143], %13, %11, s64 offen offset:3072 sc1\n"
    "s_mov_b32 s57, 1\n"
    "s_branch Lck1x_%=\n"
    "Lck1_%=:\n"
    "buffer_load_dword v60, %19, %12, 0 offen sc1\n"
    "Lck1x_%=:\n"
    "s_waitcnt lgkmcnt(9)\n"
    "v_mfma_f32_32x32x16_bf16 v[176:191], v[168:171], v[172:175], v[176:191]\n"
    "ds_read_b64_tr_b16 v[168:169], %9 offset:3584\n"
    "ds_read_b64_tr_b16 v[170:171], %10 offset:3584\n"
    "ds_read_b128 v[172:175], %18 offset:18944\n"
    "v_mov_b32 v210, 0\n"
    "v_mov_b32 v209, 0\n"
    "v_mov_b32 v208, 0\n"
    "s_waitcnt lgkmcnt(9)\n"
    "v_mfma_f32_32x32x16_bf16 v[176:191], v[144:147], v[148:151], v[176:191]\n"
    "ds_read_b64_tr_b16 v[144:145], %9 offset:7168\n"
    "ds_read_b64_tr_b16 v[146:147], %10 offset:7168\n"
    "ds_read_b128 v[148:151], %18 offset:20480\n"
    "v_mov_b32 v207, 0\n"
    "v_mov_b32 v206, 0\n"
    "v_mov_b32 v205, 0\n"
    "s_waitcnt lgkmcnt(9)\n"
    "v_mfma_f32_32x32x16_bf16 v[176:191], v[152:155], v[156:159], v[176:191]\n"
    "ds_read_b64_tr_b16 v[152:153], %9 offset:10752\n"
    "ds_read_b64_tr_b16 v[154:155], %10 offset:10752\n"
    "ds_read_b128 v[156:159], %18 offset:20992\n"
    "v_mov_b32 v204, 0\n"
    "v_mov_b32 v203, 0\n"
    "v_mov_b32 v202, 0\n"
    "s_waitcnt lgkmcnt(9)\n"
    "v_mfma_f32_32x32x16_bf16 v[176:191], v[160:163], v[164:167], v[176:191]\n"
    "ds_read_b64_tr_b16 v[160:161], %9 offset:14336\n"
    "ds_read_b64_tr_b16 v[162:163], %10 offset:14336\n"
    "ds_read_b128 v[164:167], %18 offset:22528\n"
    "v_mov_b32 v201, 0\n"
    "v_mov_b32 v200, 0\n"
    "v_mov_b32 v199, 0\n"
    "s_waitcnt lgkmcnt(9)\n"
    "v_mfma_f32_32x32x16_bf16 v[176:191], v[168:171], v[172:175], v[176:191]\n"
    "ds_read_b64_tr_b16 v[168:169], %9 offset:17920\n"
    "ds_read_b64_tr_b16 v[170:171], %10 offset:17920\n"
    "ds_read_b128 v[172:175], %18 offset:23040\n"
    "v_mov_b32 v198, 0\n"
    "v_mov_b32 v197, 0\n"
    "v_mov_b32 v196, 0\n"
    "s_waitcnt lgkmcnt(9)\n"
    "v_mfma_f32_32x32x16_bf16 v[176:191], v[144:147], v[148:151], v[176:191]\n"
    "ds_read_b64_tr_b16 v[144:145], %9 offset:21504\n"
    "ds_read_b64_tr_b16 v[146:147], %10 offset:21504\n"
    "ds_read_b128 v[148:151], %18 offset:24576\n"
    "v_mov_b32 v195, 0\n"
    "v_mov_b32 v194, 0\n"
    "v_mov_b32 v193, 0\n"
    "s_waitcnt lgkmcnt(9)\n"
    "v_mfma_f32_32x32x16_bf16 v[176:191], v[152:155], v[156:159], v[176:191]\n"
    "ds_read_b64_tr_b16 v[152:153], %9 offset:25088\n"
    "ds_read_b64_tr_b16 v[154:155], %10 offset:25088\n"
    "ds_read_b128 v[156:159], %18 offset:25088\n"
    "v_mov_b32 v192, 0\n"
    "s_waitcnt lgkmcnt(9)\n"
    "v_mfma_f32_32x32x16_bf16 v[176:191], v[160:163], v[164:167], v[176:191]\n"
    "ds_read_b64_tr_b16 v[160:161], %9 offset:28672\n"
    "ds_read_b64_tr_b16 v[162:163], %10 offset:28672\n"
    "ds_read_b128 v[164:167], %18 offset:26624\n"
    "s_waitcnt lgkmcnt(9)\n"
    "v_mfma_f32_32x32x16_bf16 v[176:191], v[168:171], v[172:175], v[176:191]\n"
    "ds_read_b64_tr_b16 v[168:169], %9 offset:32256\n"
    "ds_read_b64_tr_b16 v[170:171], %10 offset:32256\n"
    "ds_read_b128 v[172:175], %18 offset:27136\n"
    "s_waitcnt lgkmcnt(9)\n"
    "v_mfma_f32_32x32x16_bf16 v[176:191], v[144:147], v[148:151], v[176:191]\n"
    "s_waitcnt lgkmcnt(6)\n"
    "v_mfma_f32_32x32x16_bf16 v[176:191], v[152:155], v[156:159], v[176:191]\n"
    "s_waitcnt lgkmcnt(3)\n"
    "v_mfma_f32_32x32x16_bf16 v[176:191], v[160:163], v[164:167], v[176:191]\n"
    "s_waitcnt lgkmcnt(0)\n"
    "v_mfma_f32_32x32x16_bf16 v[176:191], v[168:171], v[172:175], v[176:191]\n"
    "s_cmp_lg_u32 s57, 0\n"
    "s_cbranch_scc1 Lck2x_%=\n"
    "s_mov_b32 s58, 0x100000\n"
    "Lck2_%=:\n"
    "s_waitcnt vmcnt(0)\n"
    "v_cmp_ne_u32 vcc, %20, v60\n"
    "s_nop 0\n"
    "s_cmp_eq_u64 vcc, 0\n"
    "s_cbranch_scc1 Lck2y_%=\n"
    "s_sleep 3\n"
    "buffer_load_dword v60, %19, %12, 0 offen sc1\n"
    "s_sub_u32 s58, s58, 1\n"
    "s_cbranch_scc0 Lck2_%=\n"
    "Lck2y_%=:\n"
    "buffer_load_dwordx4 v[64:67], %13, %11, s60 offen offset:0 sc1\n"
    "buffer_load_dwordx4 v[68:71], %13, %11, s60 offen offset:1024 sc1\n"
    "buffer_load_dwordx4 v[72:75], %13, %11, s60 offen offset:2048 sc1\n"
    "buffer_load_dwordx4 v[76:79], %13, %11, s60 offen offset:3072 sc1\n"
    "buffer_load_dwordx4 v[80:83], %13, %11, s61 offen offset:0 sc1\n"
    "buffer_load_dwordx4 v[84:87], %13, %11, s61 offen offset:1024 sc1\n"
    "buffer_load_dwordx4 v[88:91], %13, %11, s61 offen offset:2048 sc1\n"
    "buffer_load_dwordx4 v[92:95], %13, %11, s61 offen offset:3072 sc1\n"
    "buffer_load_dwordx4 v[96:99], %13, %11, s62 offen offset:0 sc1\n"
    "buffer_load_dwordx4 v[100:103], %13, %11, s62 offen offset:1024 sc1\n"
    "buffer_load_dwordx4 v[104:107], %13, %11, s62 offen offset:2048 sc1\n"
    "buffer_load_dwordx4 v[108:111], %13, %11, s62 offen offset:3072 sc1\n"
    "buffer_load_dwordx4 v[112:115], %13, %11, s63 offen offset:0 sc1\n"
    "buffer_load_dwordx4 v[116:119], %13, %11, s63 offen offset:1024 sc1\n"
    "buffer_load_dwordx4 v[120:123], %13, %11, s63 offen offset:2048 sc1\n"
    "buffer_load_dwordx4 v[124:127], %13, %11, s63 offen offset:3072 sc1\n"
    "buffer_load_dwordx4 v[128:131], %13, %11, s64 offen offset:0 sc1\n"
    "buffer_load_dwordx4 v[132:135], %13, %11, s64 offen offset:1024 sc1\n"
    "buffer_load_dwordx4 v[136:139], %13, %11, s64 offen offset:2048 sc1\n"
    "buffer_load_dwordx4 v[140:143], %13, %11, s64 offen offset:3072 sc1\n"
    "Lck2x_%=:\n"
    "s_nop 9\n"
    "s_mov_b32 s56, 0x3e8b97f6\n"
    "v_mul_f32 v176, s56, v176\n"
    "v_mul_f32 v177, s56, v177\n"
    "v_mul_f32 v178, s56, v178\n"
    "v_mul_f32 v179, s56, v179\n"
    "v_mul_f32 v180, s56, v180\n"
    "v_mul_f32 v181, s56, v181\n"
    "v_mul_f32 v182, s56, v182\n"
    "v_mul_f32 v183, s56, v183\n"
    "v_mul_f32 v184, s56, v184\n"
    "v_mul_f32 v185, s56, v185\n"
    "v_mul_f32 v186, s56, v186\n"
    "v_mul_f32 v187, s56, v187\n"
    "v_mul_f32 v188, s56, v188\n"
    "v_mul_f32 v189, s56, v189\n"
    "v_mul_f32 v190, s56, v190\n"
    "v_mul_f32 v191, s56, v191\n"
    "v_cvt_pk_bf16_f32 v144, v176, v177\n"
    "v_cvt_pk_bf16_f32 v145, v178, v179\n"
    "v_cvt_pk_bf16_f32 v146, v180, v181\n"
    "v_cvt_pk_bf16_f32 v147, v182, v183\n"
    "v_cvt_pk_bf16_f32 v148, v184, v185\n"
    "v_cvt_pk_bf16_f32 v149, v186, v187\n"
    "v_cvt_pk_bf16_f32 v150, v188, v189\n"
    "v_cvt_pk_bf16_f32 v151, v190, v191\n"
    "s_mov_b32 exec_lo, 0\n"
    "v_mov_b32 v150, 0\n"
    "v_mov_b32 v151, 0\n"
    "s_mov_b32 exec_lo, -1\n"
    "s_mov_b32 s58, 0x100000\n"
    "Lq3_%=:\n"
    "ds_read_b32 v62, %21 offset:16\n"
    "s_waitcnt lgkmcnt(0)\n"
    "v_readfirstlane_b32 s59, v62\n"
    "s_cmp_ge_u32 s59, 4\n"
    "s_cbranch_scc1 Lq3x_%=\n"
    "s_sub_u32 s58, s58, 1\n"
    "s_cbranch_scc0 Lq3_%=\n"
    "Lq3x_%=:\n"
    "ds_write_b128 %22, v[144:147]\n"
    "ds_write_b128 %22, v[148:151] offset:1024\n"
    "s_mov_b64 exec, 1\n"
    "ds_add_u32 %21, v61 offset:20\n"
    "s_mov_b64 exec, -1\n"
    "s_mov_b32 s58, 0x100000\n"
    "Lqx_%=:\n"
    "ds_read_b32 v62, %21 offset:20\n"
    "s_waitcnt lgkmcnt(0)\n"
    "v_readfirstlane_b32 s59, v62\n"
    "s_cmp_ge_u32 s59, 4\n"
    "s_cbranch_scc1 Lqxx_%=\n"
    "s_sub_u32 s58, s58, 1\n"
    "s_cbranch_scc0 Lqx_%=\n"
    "Lqxx_%=:\n"
    "ds_read_b128 %0, %14 offset:0\n"
    "ds_read_b128 %1, %14 offset:1024\n"
    "ds_read_b128 %2, %14 offset:2048\n"
    "ds_read_b128 %3, %14 offset:3072\n"
    "ds_read_b128 %4, %14 offset:4096\n"
    "ds_read_b128 %5, %14 offset:5120\n"
    "ds_read_b128 %6, %14 offset:6144\n"
    "ds_read_b128 %7, %14 offset:7168\n"
    "s_mov_b64 exec, 1\n"
    "ds_add_u32 %21, v61 offset:24\n"
    "s_mov_b64 exec, -1\n"
    "s_waitcnt lgkmcnt(0)\n"
    "Lnoq_%=:\n"
    "s_setprio 2\n"
    "s_waitcnt vmcnt(19)\n"
    "v_mfma_f32_32x32x16_bf16 v[144:159], v[64:67], %0, 0\n"
    "s_waitcnt vmcnt(18)\n"
    "v_mfma_f32_32x32x16_bf16 v[144:159], v[68:71], %1, v[144:159]\n"
    "s_nop 7\n"
    "s_nop 3\n"
    "v_mfma_f32_32x32x16_bf16 v[160:175], v[64:67], %2, 0\n"
    "v_exp_f32 v144, v144\n"
    "v_exp_f32 v145, v145\n"
    "v_exp_f32 v146, v146\n"
    "v_mfma_f32_32x32x16_bf16 v[160:175], v[68:71], %3, v[160:175]\n"
    "v_exp_f32 v147, v147\n"
    "v_exp_f32 v148, v148\n"
    "v_exp_f32 v149, v149\n"
    "v_exp_f32 v150, v150\n"
    "v_exp_f32 v151, v151\n"
    "v_exp_f32 v152, v152\n"
    "v_exp_f32 v153, v153\n"
    "s_waitcnt vmcnt(17)\n"
    "v_smfmac_f32_32x32x32_bf16 v[192:207], v[72:75], v[144:151], v63\n"
    "v_exp_f32 v154, v154\n"
    "v_exp_f32 v155, v155\n"
    "v_exp_f32 v156, v156\n"
    "v_exp_f32 v157, v157\n"
    "v_exp_f32 v158, v158\n"
    "v_exp_f32 v159, v159\n"
    "v_mfma_f32_32x32x16_bf16 v[176:191], v[64:67], %4, 0\n"
    "v_exp_f32 v160, v160\n"
    "v_exp_f32 v161, v161\n"
    "v_exp_f32 v162, v162\n"
    "v_mfma_f32_32x32x16_bf16 v[176:191], v[68:71], %5, v[176:191]\n"
    "v_exp_f32 v163, v163\n"
    "v_exp_f32 v164, v164\n"
    "v_exp_f32 v165, v165\n"
    "s_waitcnt vmcnt(16)\n"
    "v_smfmac_f32_32x32x32_bf16 v[192:207], v[76:79], v[152:159], v63\n"
    "v_exp_f32 v166, v166\n"
    "v_exp_f32 v167, v167\n"
    "v_exp_f32 v168, v168\n"
    "v_exp_f32 v169, v169\n"
    "v_smfmac_f32_32x32x32_bf16 v[208:223], v[72:75], v[160:167], v63\n"
    "v_exp_f32 v170, v170\n"
    "v_exp_f32 v171, v171\n"
    "v_exp_f32 v172, v172\n"
    "v_exp_f32 v173, v173\n"
    "v_exp_f32 v174, v174\n"
    "v_exp_f32 v175, v175\n"
    "v_mfma_f32_32x32x16_bf16 v[144:159], v[64:67], %6, 0\n"
    "v_exp_f32 v176, v176\n"
    "v_exp_f32 v177, v177\n"
    "v_exp_f32 v178, v178\n"
    "v_mfma_f32_32x32x16_bf16 v[144:159], v[68:71], %7, v[144:159]\n"
    "v_exp_f32 v179, v179\n"
    "v_exp_f32 v180, v180\n"
    "v_exp_f32 v181, v181\n"
    "v_smfmac_f32_32x32x32_bf16 v[208:223], v[76:79], v[168:175], v63\n"
    "v_exp_f32 v182, v182\n"
    "v_exp_f32 v183, v183\n"
    "v_exp_f32 v184, v184\n"
    "v_exp_f32 v185, v185\n"
    "v_smfmac_f32_32x32x32_bf16 v[224:239], v[72:75], v[176:183], v63\n"
    "v_exp_f32 v186, v186\n"
    "v_exp_f32 v187, v187\n"
    "v_exp_f32 v188, v188\n"
    "v_exp_f32 v189, v189\n"
    "v_exp_f32 v190, v190\n"
    "v_exp_f32 v191, v191\n"
    "s_waitcnt vmcnt(12)\n"
    "v_mfma_f32_32x32x16_bf16 v[160:175], v[80:83], %0, 0\n"
    "v_exp_f32 v144, v144\n"
    "v_exp_f32 v145, v145\n"
    "v_exp_f32 v146, v146\n"
    "v_mfma_f32_32x32x16_bf16 v[160:175], v[84:87], %1, v[160:175]\n"
    "v_exp_f32 v147, v147\n"
    "v_exp_f32 v148, v148\n"
    "v_exp_f32 v149, v149\n"
    "v_smfmac_f32_32x32x32_bf16 v[224:239], v[76:79], v[184:191], v63\n"
    "v_exp_f32 v150, v150\n"
    "v_exp_f32 v151, v151\n"
    "v_exp_f32 v152, v152\n"
    "v_exp_f32 v153, v153\n"
    "v_smfmac_f32_32x32x32_bf16 v[240:255], v[72:75], v[144:151], v63\n"
    "v_exp_f32 v154, v154\n"
    "v_exp_f32 v155, v155\n"
    "v_exp_f32 v156, v156\n"
    "v_exp_f32 v157, v157\n"
    "v_exp_f32 v158, v158\n"
    "v_exp_f32 v159, v159\n"
    "v_mfma_f32_32x32x16_bf16 v[176:191], v[80:83], %2, 0\n"
    "buffer_load_dwordx4 v[64:67], %13, %11, s65 offen offset:0 sc1\n"
    "v_exp_f32 v160, v160\n"
    "v_exp_f32 v161, v161\n"
    "v_exp_f32 v162, v162\n"
    "v_mfma_f32_32x32x16_bf16 v[176:191], v[84:87], %3, v[176:191]\n"
    "buffer_load_dwordx4 v[68:71], %13, %11, s65 offen offset:1024 sc1\n"
    "v_exp_f32 v163, v163\n"
    "v_exp_f32 v164, v164\n"
    "v_exp_f32 v165, v165\n"
    "v_smfmac_f32_32x32x32_bf16 v[240:255], v[76:79], v[152:159], v63\n"
    "buffer_load_dwordx4 v[72:75], %13, %11, s65 offen offset:2048 sc1\n"
    "v_exp_f32 v166, v166\n"
    "v_exp_f32 v167, v167\n"
    "v_exp_f32 v168, v168\n"
    "v_exp_f32 v169, v169\n"
    "v_smfmac_f32_32x32x32_bf16 v[192:207], v[88:91], v[160:167], v63\n"
    "buffer_load_dwordx4 v[76:79], %13, %11, s65 offen offset:3072 sc1\n"
    "v_exp_f32 v170, v170\n"
    "v_exp_f32 v171, v171\n"
    "v_exp_f32 v172, v172\n"
    "v_exp_f32 v173, v173\n"
    "v_exp_f32 v174, v174\n"
    "v_exp_f32 v175, v175\n"
    "v_mfma_f32_32x32x16_bf16 v[144:159], v[80:83], %4, 0\n"
    "v_exp_f32 v176, v176\n"
    "v_exp_f32 v177, v177\n"
    "v_exp_f32 v178, v178\n"
    "v_mfma_f32_32x32x16_bf16 v[144:159], v[84:87], %5, v[144:159]\n"
    "v_exp_f32 v179, v179\n"
    "v_exp_f32 v180, v180\n"
    "v_exp_f32 v181, v181\n"
    "v_smfmac_f32_32x32x32_bf16 v[192:207], v[92:95], v[168:175], v63\n"
    "v_exp_f32 v182, v182\n"
    "v_exp_f32 v183, v183\n"
    "v_exp_f32 v184, v184\n"
    "v_exp_f32 v185, v185\n"
    "v_smfmac_f32_32x32x32_bf16 v[208:223], v[88:91], v[176:183], v63\n"
    "v_exp_f32 v186, v186\n"
    "v_exp_f32 v187, v187\n"
    "v_exp_f32 v188, v188\n"
    "v_exp_f32 v189, v189\n"
    "v_exp_f32 v190, v190\n"
    "v_exp_f32 v191, v191\n"
    "v_mfma_f32_32x32x16_bf16 v[160:175], v[80:83], %6, 0\n"
    "v_exp_f32 v144, v144\n"
    "v_exp_f32 v145, v145\n"
    "v_exp_f32 v146, v146\n"
    "v_mfma_f32_32x32x16_bf16 v[160:175], v[84:87], %7, v[160:175]\n"
    "v_exp_f32 v147, v147\n"
    "v_exp_f32 v148, v148\n"
    "v_exp_f32 v149, v149\n"
    "v_smfmac_f32_32x32x32_bf16 v[208:223], v[92:95], v[184:191], v63\n"
    "v_exp_f32 v150, v150\n"
    "v_exp_f32 v151, v151\n"
    "v_exp_f32 v152, v152\n"
    "v_exp_f32 v153, v153\n"
    "v_smfmac_f32_32x32x32_bf16 v[224:239], v[88:91], v[144:151], v63\n"
    "v_exp_f32 v154, v154\n"
    "v_exp_f32 v155, v155\n"
    "v_exp_f32 v156, v156\n"
    "v_exp_f32 v157, v157\n"
    "v_exp_f32 v158, v158\n"
    "v_exp_f32 v159, v159\n"
    "s_waitcnt vmcnt(12)\n"
    "v_mfma_f32_32x32x16_bf16 v[176:191], v[96:99], %0, 0\n"
    "v_exp_f32 v160, v160\n"
    "v_exp_f32 v161, v161\n"
    "v_exp_f32 v162, v162\n"
    "v_mfma_f32_32x32x16_bf16 v[176:191], v[100:103], %1, v[176:191]\n"
    "v_exp_f32 v163, v163\n"
    "v_exp_f32 v164, v164\n"
    "v_exp_f32 v165, v165\n"
    "v_smfmac_f32_32x32x32_bf16 v[224:239], v[92:95], v[152:159], v63\n"
    "v_exp_f32 v166, v166\n"
    "v_exp_f32 v167, v167\n"
    "v_exp_f32 v168, v168\n"
    "v_exp_f32 v169, v169\n"
    "v_smfmac_f32_32x32x32_bf16 v[240:255], v[88:91], v[160:167], v63\n"
    "v_exp_f32 v170, v170\n"
    "v_exp_f32 v171, v171\n"
    "v_exp_f32 v172, v172\n"
    "v_exp_f32 v173, v173\n"
    "v_exp_f32 v174, v174\n"
    "v_exp_f32 v175, v175\n"
    "v_mfma_f32_32x32x16_bf16 v[144:159], v[96:99], %2, 0\n"
    "buffer_load_dwordx4 v[80:83], %13, %11, s66 offen offset:0 sc1\n"
    "v_exp_f32 v176, v176\n"
    "v_exp_f32 v177, v177\n"
    "v_exp_f32 v178, v178\n"
    "v_mfma_f32_32x32x16_bf16 v[144:159], v[100:103], %3, v[144:159]\n"
    "buffer_load_dwordx4 v[84:87], %13, %11, s66 offen offset:1024 sc1\n"
    "v_exp_f32 v179, v179\n"
    "v_exp_f32 v180, v180\n"
    "v_exp_f32 v181, v181\n"
    "v_smfmac_f32_32x32x32_bf16 v[240:255], v[92:95], v[168:175], v63\n"
    "buffer_load_dwordx4 v[88:91], %13, %11, s66 offen offset:2048 sc1\n"
    "v_exp_f32 v182, v182\n"
    "v_exp_f32 v183, v183\n"
    "v_exp_f32 v184, v184\n"
    "v_exp_f32 v185, v185\n"
    "v_smfmac_f32_32x32x32_bf16 v[192:207], v[104:107], v[176:183], v63\n"
    "buffer_load_dwordx4 v[92:95], %13, %11, s66 offen offset:3072 sc1\n"
    "v_exp_f32 v186, v186\n"
    "v_exp_f32 v187, v187\n"
    "v_exp_f32 v188, v188\n"
    "v_exp_f32 v189, v189\n"
    "v_exp_f32 v190, v190\n"
    "v_exp_f32 v191, v191\n"
    "v_mfma_f32_32x32x16_bf16 v[160:175], v[96:99], %4, 0\n"
    "v_exp_f32 v144, v144\n"
    "v_exp_f32 v145, v145\n"
    "v_exp_f32 v146, v146\n"
    "v_mfma_f32_32x32x16_bf16 v[160:175], v[100:103], %5, v[160:175]\n"
    "v_exp_f32 v147, v147\n"
    "v_exp_f32 v148, v148\n"
    "v_exp_f32 v149, v149\n"
    "v_smfmac_f32_32x32x32_bf16 v[192:207], v[108:111], v[184:191], v63\n"
    "v_exp_f32 v150, v150\n"
    "v_exp_f32 v151, v151\n"
    "v_exp_f32 v152, v152\n"
    "v_exp_f32 v153, v153\n"
    "v_smfmac_f32_32x32x32_bf16 v[208:223], v[104:107], v[144:151], v63\n"
    "v_exp_f32 v154, v154\n"
    "v_exp_f32 v155, v155\n"
    "v_exp_f32 v156, v156\n"
    "v_exp_f32 v157, v157\n"
    "v_exp_f32 v158, v158\n"
    "v_exp_f32 v159, v159\n"
    "v_mfma_f32_32x32x16_bf16 v[176:191], v[96:99], %6, 0\n"
    "v_exp_f32 v160, v160\n"
    "v_exp_f32 v161, v161\n"
    "v_exp_f32 v162, v162\n"
    "v_mfma_f32_32x32x16_bf16 v[176:191], v[100:103], %7, v[176:191]\n"
    "v_exp_f32 v163, v163\n"
    "v_exp_f32 v164, v164\n"
    "v_exp_f32 v165, v165\n"
    "v_smfmac_f32_32x32x32_bf16 v[208:223], v[108:111], v[152:159], v63\n"
    "v_exp_f32 v166, v166\n"
    "v_exp_f32 v167, v167\n"
    "v_exp_f32 v168, v168\n"
    "v_exp_f32 v169, v169\n"
    "v_smfmac_f32_32x32x32_bf16 v[224:239], v[104:107], v[160:167], v63\n"
    "v_exp_f32 v170, v170\n"
    "v_exp_f32 v171, v171\n"
    "v_exp_f32 v172, v172\n"
    "v_exp_f32 v173, v173\n"
    "v_exp_f32 v174, v174\n"
    "v_exp_f32 v175, v175\n"
    "s_waitcnt vmcnt(12)\n"
    "v_mfma_f32_32x32x16_bf16 v[144:159], v[112:115], %0, 0\n"
    "v_exp_f32 v176, v176\n"
    "v_exp_f32 v177, v177\n"
    "v_exp_f32 v178, v178\n"
    "v_mfma_f32_32x32x16_bf16 v[144:159], v[116:119], %1, v[144:159]\n"
    "v_exp_f32 v179, v179\n"
    "v_exp_f32 v180, v180\n"
    "v_exp_f32 v181, v181\n"
    "v_smfmac_f32_32x32x32_bf16 v[224:239], v[108:111], v[168:175], v63\n"
    "v_exp_f32 v182, v182\n"
    "v_exp_f32 v183, v183\n"
    "v_exp_f32 v184, v184\n"
    "v_exp_f32 v185, v185\n"
    "v_smfmac_f32_32x32x32_bf16 v[240:255], v[104:107], v[176:183], v63\n"
    "v_exp_f32 v186, v186\n"
    "v_exp_f32 v187, v187\n"
    "v_exp_f32 v188, v188\n"
    "v_exp_f32 v189, v189\n"
    "v_exp_f32 v190, v190\n"
    "v_exp_f32 v191, v191\n"
    "v_mfma_f32_32x32x16_bf16 v[160:175], v[112:115], %2, 0\n"
    "buffer_load_dwordx4 v[96:99], %13, %11, s67 offen offset:0 sc1\n"
    "v_exp_f32 v144, v144\n"
    "v_exp_f32 v145, v145\n"
    "v_exp_f32 v146, v146\n"
    "v_mfma_f32_32x32x16_bf16 v[160:175], v[116:119], %3, v[160:175]\n"
    "buffer_load_dwordx4 v[100:103], %13, %11, s67 offen offset:1024 sc1\n"
    "v_exp_f32 v147, v147\n"
    "v_exp_f32 v148, v148\n"
    "v_exp_f32 v149, v149\n"
    "v_smfmac_f32_32x32x32_bf16 v[240:255], v[108:111], v[184:191], v63\n"
    "buffer_load_dwordx4 v[104:107], %13, %11, s67 offen offset:2048 sc1\n"
    "v_exp_f32 v150, v150\n"
    "v_exp_f32 v151, v151\n"
    "v_exp_f32 v152, v152\n"
    "v_exp_f32 v153, v153\n"
    "v_smfmac_f32_32x32x32_bf16 v[192:207], v[120:123], v[144:151], v63\n"
    "buffer_load_dwordx4 v[108:111], %13, %11, s67 offen offset:3072 sc1\n"
    "v_exp_f32 v154, v154\n"
    "v_exp_f32 v155, v155\n"
    "v_exp_f32 v156, v156\n"
    "v_exp_f32 v157, v157\n"
    "v_exp_f32 v158, v158\n"
    "v_exp_f32 v159, v159\n"
    "v_mfma_f32_32x32x16_bf16 v[176:191], v[112:115], %4, 0\n"
    "v_exp_f32 v160, v160\n"
    "v_exp_f32 v161, v161\n"
    "v_exp_f32 v162, v162\n"
    "v_mfma_f32_32x32x16_bf16 v[176:191], v[116:119], %5, v[176:191]\n"
    "v_exp_f32 v163, v163\n"
    "v_exp_f32 v164, v164\n"
    "v_exp_f32 v165, v165\n"
    "v_smfmac_f32_32x32x32_bf16 v[192:207], v[124:127], v[152:159], v63\n"
    "v_exp_f32 v166, v166\n"
    "v_exp_f32 v167, v167\n"
    "v_exp_f32 v168, v168\n"
    "v_exp_f32 v169, v169\n"
    "v_smfmac_f32_32x32x32_bf16 v[208:223], v[120:123], v[160:167], v63\n"
    "v_exp_f32 v170, v170\n"
    "v_exp_f32 v171, v171\n"
    "v_exp_f32 v172, v172\n"
    "v_exp_f32 v173, v173\n"
    "v_exp_f32 v174, v174\n"
    "v_exp_f32 v175, v175\n"
    "v_mfma_f32_32x32x16_bf16 v[144:159], v[112:115], %6, 0\n"
    "v_exp_f32 v176, v176\n"
    "v_exp_f32 v177, v177\n"
    "v_exp_f32 v178, v178\n"
    "v_mfma_f32_32x32x16_bf16 v[144:159], v[116:119], %7, v[144:159]\n"
    "v_exp_f32 v179, v179\n"
    "v_exp_f32 v180, v180\n"
    "v_exp_f32 v181, v181\n"
    "v_smfmac_f32_32x32x32_bf16 v[208:223], v[124:127], v[168:175], v63\n"
    "v_exp_f32 v182, v182\n"
    "v_exp_f32 v183, v183\n"
    "v_exp_f32 v184, v184\n"
    "v_exp_f32 v185, v185\n"
    "v_smfmac_f32_32x32x32_bf16 v[224:239], v[120:123], v[176:183], v63\n"
    "v_exp_f32 v186, v186\n"
    "v_exp_f32 v187, v187\n"
    "v_exp_f32 v188, v188\n"
    "v_exp_f32 v189, v189\n"
    "v_exp_f32 v190, v190\n"
    "v_exp_f32 v191, v191\n"
    "s_waitcnt vmcnt(12)\n"
    "v_mfma_f32_32x32x16_bf16 v[160:175], v[128:131], %0, 0\n"
    "v_exp_f32 v144, v144\n"
    "v_exp_f32 v145, v145\n"
    "v_exp_f32 v146, v146\n"
    "v_mfma_f32_32x32x16_bf16 v[160:175], v[132:135], %1, v[160:175]\n"
    "v_exp_f32 v147, v147\n"
    "v_exp_f32 v148, v148\n"
    "v_exp_f32 v149, v149\n"
    "v_smfmac_f32_32x32x32_bf16 v[224:239], v[124:127], v[184:191], v63\n"
    "v_exp_f32 v150, v150\n"
    "v_exp_f32 v151, v151\n"
    "v_exp_f32 v152, v152\n"
    "v_exp_f32 v153, v153\n"
    "v_smfmac_f32_32x32x32_bf16 v[240:255], v[120:123], v[144:151], v63\n"
    "v_exp_f32 v154, v154\n"
    "v_exp_f32 v155, v155\n"
    "v_exp_f32 v156, v156\n"
    "v_exp_f32 v157, v157\n"
    "v_exp_f32 v158, v158\n"
    "v_exp_f32 v159, v159\n"
    "v_mfma_f32_32x32x16_bf16 v[176:191], v[128:131], %2, 0\n"
    "buffer_load_dwordx4 v[112:115], %13, %11, s68 offen offset:0 sc1\n"
    "v_exp_f32 v160, v160\n"
    "v_exp_f32 v161, v161\n"
    "v_exp_f32 v162, v162\n"
    "v_mfma_f32_32x32x16_bf16 v[176:191], v[132:135], %3, v[176:191]\n"
    "buffer_load_dwordx4 v[116:119], %13, %11, s68 offen offset:1024 sc1\n"
    "v_exp_f32 v163, v163\n"
    "v_exp_f32 v164, v164\n"
    "v_exp_f32 v165, v165\n"
    "v_smfmac_f32_32x32x32_bf16 v[240:255], v[124:127], v[152:159], v63\n"
    "buffer_load_dwordx4 v[120:123], %13, %11, s68 offen offset:2048 sc1\n"
    "v_exp_f32 v166, v166\n"
    "v_exp_f32 v167, v167\n"
    "v_exp_f32 v168, v168\n"
    "v_exp_f32 v169, v169\n"
    "v_smfmac_f32_32x32x32_bf16 v[192:207], v[136:139], v[160:167], v63\n"
    "buffer_load_dwordx4 v[124:127], %13, %11, s68 offen offset:3072 sc1\n"
    "v_exp_f32 v170, v170\n"
    "v_exp_f32 v171, v171\n"
    "v_exp_f32 v172, v172\n"
    "v_exp_f32 v173, v173\n"
    "v_exp_f32 v174, v174\n"
    "v_exp_f32 v175, v175\n"
    "v_mfma_f32_32x32x16_bf16 v[144:159], v[128:131], %4, 0\n"
    "v_exp_f32 v176, v176\n"
    "v_exp_f32 v177, v177\n"
    "v_exp_f32 v178, v178\n"
    "v_mfma_f32_32x32x16_bf16 v[144:159], v[132:135], %5, v[144:159]\n"
    "v_exp_f32 v179, v179\n"
    "v_exp_f32 v180, v180\n"
    "v_exp_f32 v181, v181\n"
    "v_smfmac_f32_32x32x32_bf16 v[192:207], v[140:143], v[168:175], v63\n"
    "v_exp_f32 v182, v182\n"
    "v_exp_f32 v183, v183\n"
    "v_exp_f32 v184, v184\n"
    "v_exp_f32 v185, v185\n"
    "v_smfmac_f32_32x32x32_bf16 v[208:223], v[136:139], v[176:183], v63\n"
    "v_exp_f32 v186, v186\n"
    "v_exp_f32 v187, v187\n"
    "v_exp_f32 v188, v188\n"
    "v_exp_f32 v189, v189\n"
    "v_exp_f32 v190, v190\n"
    "v_exp_f32 v191, v191\n"
    "v_mfma_f32_32x32x16_bf16 v[160:175], v[128:131], %6, 0\n"
    "v_exp_f32 v144, v144\n"
    "v_exp_f32 v145, v145\n"
    "v_exp_f32 v146, v146\n"
    "v_mfma_f32_32x32x16_bf16 v[160:175], v[132:135], %7, v[160:175]\n"
    "v_exp_f32 v147, v147\n"
    "v_exp_f32 v148, v148\n"
    "v_exp_f32 v149, v149\n"
    "v_smfmac_f32_32x32x32_bf16 v[208:223], v[140:143], v[184:191], v63\n"
    "v_exp_f32 v150, v150\n"
    "v_exp_f32 v151, v151\n"
    "v_exp_f32 v152, v152\n"
    "v_exp_f32 v153, v153\n"
    "v_smfmac_f32_32x32x32_bf16 v[224:239], v[136:139], v[144:151], v63\n"
    "v_exp_f32 v154, v154\n"
    "v_exp_f32 v155, v155\n"
    "v_exp_f32 v156, v156\n"
    "v_exp_f32 v157, v157\n"
    "v_exp_f32 v158, v158\n"
    "v_exp_f32 v159, v159\n"
    "s_waitcnt vmcnt(12)\n"
    "v_mfma_f32_32x32x16_bf16 v[176:191], v[64:67], %0, 0\n"
    "v_exp_f32 v160, v160\n"
    "v_exp_f32 v161, v161\n"
    "v_exp_f32 v162, v162\n"
    "v_mfma_f32_32x32x16_bf16 v[176:191], v[68:71], %1, v[176:191]\n"
    "v_exp_f32 v163, v163\n"
    "v_exp_f32 v164, v164\n"
    "v_exp_f32 v165, v165\n"
    "v_smfmac_f32_32x32x32_bf16 v[224:239], v[140:143], v[152:159], v63\n"
    "v_exp_f32 v166, v166\n"
    "v_exp_f32 v167, v167\n"
    "v_exp_f32 v168, v168\n"
    "v_exp_f32 v169, v169\n"
    "v_smfmac_f32_32x32x32_bf16 v[240:255], v[136:139], v[160:167], v63\n"
    "v_exp_f32 v170, v170\n"
    "v_exp_f32 v171, v171\n"
    "v_exp_f32 v172, v172\n"
    "v_exp_f32 v173, v173\n"
    "v_exp_f32 v174, v174\n"
    "v_exp_f32 v175, v175\n"
    "v_mfma_f32_32x32x16_bf16 v[144:159], v[64:67], %2, 0\n"
    "buffer_load_dwordx4 v[128:131], %13, %11, s69 offen offset:0 sc1\n"
    "v_exp_f32 v176, v176\n"
    "v_exp_f32 v177, v177\n"
    "v_exp_f32 v178, v178\n"
    "v_mfma_f32_32x32x16_bf16 v[144:159], v[68:71], %3, v[144:159]\n"
    "buffer_load_dwordx4 v[132:135], %13, %11, s69 offen offset:1024 sc1\n"
    "v_exp_f32 v179, v179\n"
    "v_exp_f32 v180, v180\n"
    "v_exp_f32 v181, v181\n"
    "v_smfmac_f32_32x32x32_bf16 v[240:255], v[140:143], v[168:175], v63\n"
    "buffer_load_dwordx4 v[136:139], %13, %11, s69 offen offset:2048 sc1\n"
    "v_exp_f32 v182, v182\n"
    "v_exp_f32 v183, v183\n"
    "v_exp_f32 v184, v184\n"
    "v_exp_f32 v185, v185\n"
    "v_smfmac_f32_32x32x32_bf16 v[192:207], v[72:75], v[176:183], v63\n"
    "buffer_load_dwordx4 v[140:143], %13, %11, s69 offen offset:3072 sc1\n"
    "v_exp_f32 v186, v186\n"
    "v_exp_f32 v187, v187\n"
    "v_exp_f32 v188, v188\n"
    "v_exp_f32 v189, v189\n"
    "v_exp_f32 v190, v190\n"
    "v_exp_f32 v191, v191\n"
    "v_mfma_f32_32x32x16_bf16 v[160:175], v[64:67], %4, 0\n"
    "v_exp_f32 v144, v144\n"
    "v_exp_f32 v145, v145\n"
    "v_exp_f32 v146, v146\n"
    "v_mfma_f32_32x32x16_bf16 v[160:175], v[68:71], %5, v[160:175]\n"
    "v_exp_f32 v147, v147\n"
    "v_exp_f32 v148, v148\n"
    "v_exp_f32 v149, v149\n"
    "v_smfmac_f32_32x32x32_bf16 v[192:207], v[76:79], v[184:191], v63\n"
    "v_exp_f32 v150, v150\n"
    "v_exp_f32 v151, v151\n"
    "v_exp_f32 v152, v152\n"
    "v_exp_f32 v153, v153\n"
    "v_smfmac_f32_32x32x32_bf16 v[208:223], v[72:75], v[144:151], v63\n"
    "v_exp_f32 v154, v154\n"
    "v_exp_f32 v155, v155\n"
    "v_exp_f32 v156, v156\n"
    "v_exp_f32 v157, v157\n"
    "v_exp_f32 v158, v158\n"
    "v_exp_f32 v159, v159\n"
    "v_mfma_f32_32x32x16_bf16 v[176:191], v[64:67], %6, 0\n"
    "v_exp_f32 v160, v160\n"
    "v_exp_f32 v161, v161\n"
    "v_exp_f32 v162, v162\n"
    "v_mfma_f32_32x32x16_bf16 v[176:191], v[68:71], %7, v[176:191]\n"
    "v_exp_f32 v163, v163\n"
    "v_exp_f32 v164, v164\n"
    "v_exp_f32 v165, v165\n"
    "v_smfmac_f32_32x32x32_bf16 v[208:223], v[76:79], v[152:159], v63\n"
    "v_exp_f32 v166, v166\n"
    "v_exp_f32 v167, v167\n"
    "v_exp_f32 v168, v168\n"
    "v_exp_f32 v169, v169\n"
    "v_smfmac_f32_32x32x32_bf16 v[224:239], v[72:75], v[160:167], v63\n"
    "v_exp_f32 v170, v170\n"
    "v_exp_f32 v171, v171\n"
    "v_exp_f32 v172, v172\n"
    "v_exp_f32 v173, v173\n"
    "v_exp_f32 v174, v174\n"
    "v_exp_f32 v175, v175\n"
    "s_waitcnt vmcnt(12)\n"
    "v_mfma_f32_32x32x16_bf16 v[144:159], v[80:83], %0, 0\n"
    "v_exp_f32 v176, v176\n"
    "v_exp_f32 v177, v177\n"
    "v_exp_f32 v178, v178\n"
    "v_mfma_f32_32x32x16_bf16 v[144:159], v[84:87], %1, v[144:159]\n"
    "v_exp_f32 v179, v179\n"
    "v_exp_f32 v180, v180\n"
    "v_exp_f32 v181, v181\n"
    "v_smfmac_f32_32x32x32_bf16 v[224:239], v[76:79], v[168:175], v63\n"
    "v_exp_f32 v182, v182\n"
    "v_exp_f32 v183, v183\n"
    "v_exp_f32 v184, v184\n"
    "v_exp_f32 v185, v185\n"
    "v_smfmac_f32_32x32x32_bf16 v[240:255], v[72:75], v[176:183], v63\n"
    "v_exp_f32 v186, v186\n"
    "v_exp_f32 v187, v187\n"
    "v_exp_f32 v188, v188\n"
    "v_exp_f32 v189, v189\n"
    "v_exp_f32 v190, v190\n"
    "v_exp_f32 v191, v191\n"
    "v_mfma_f32_32x32x16_bf16 v[160:175], v[80:83], %2, 0\n"
    "buffer_load_dwordx4 v[64:67], %13, %11, s70 offen offset:0 sc1\n"
    "v_exp_f32 v144, v144\n"
    "v_exp_f32 v145, v145\n"
    "v_exp_f32 v146, v146\n"
    "v_mfma_f32_32x32x16_bf16 v[160:175], v[84:87], %3, v[160:175]\n"
    "buffer_load_dwordx4 v[68:71], %13, %11, s70 offen offset:1024 sc1\n"
    "v_exp_f32 v147, v147\n"
    "v_exp_f32 v148, v148\n"
    "v_exp_f32 v149, v149\n"
    "v_smfmac_f32_32x32x32_bf16 v[240:255], v[76:79], v[184:191], v63\n"
    "buffer_load_dwordx4 v[72:75], %13, %11, s70 offen offset:2048 sc1\n"
    "v_exp_f32 v150, v150\n"
    "v_exp_f32 v151, v151\n"
    "v_exp_f32 v152, v152\n"
    "v_exp_f32 v153, v153\n"
    "v_smfmac_f32_32x32x32_bf16 v[192:207], v[88:91], v[144:151], v63\n"
    "buffer_load_dwordx4 v[76:79], %13, %11, s70 offen offset:3072 sc1\n"
    "v_exp_f32 v154, v154\n"
    "v_exp_f32 v155, v155\n"
    "v_exp_f32 v156, v156\n"
    "v_exp_f32 v157, v157\n"
    "v_exp_f32 v158, v158\n"
    "v_exp_f32 v159, v159\n"
    "v_mfma_f32_32x32x16_bf16 v[176:191], v[80:83], %4, 0\n"
    "v_exp_f32 v160, v160\n"
    "v_exp_f32 v161, v161\n"
    "v_exp_f32 v162, v162\n"
    "v_mfma_f32_32x32x16_bf16 v[176:191], v[84:87], %5, v[176:191]\n"
    "v_exp_f32 v163, v163\n"
    "v_exp_f32 v164, v164\n"
    "v_exp_f32 v165, v165\n"
    "v_smfmac_f32_32x32x32_bf16 v[192:207], v[92:95], v[152:159], v63\n"
    "v_exp_f32 v166, v166\n"
    "v_exp_f32 v167, v167\n"
    "v_exp_f32 v168, v168\n"
    "v_exp_f32 v169, v169\n"
    "v_smfmac_f32_32x32x32_bf16 v[208:223], v[88:91], v[160:167], v63\n"
    "v_exp_f32 v170, v170\n"
    "v_exp_f32 v171, v171\n"
    "v_exp_f32 v172, v172\n"
    "v_exp_f32 v173, v173\n"
    "v_exp_f32 v174, v174\n"
    "v_exp_f32 v175, v175\n"
    "v_mfma_f32_32x32x16_bf16 v[144:159], v[80:83], %6, 0\n"
    "v_exp_f32 v176, v176\n"
    "v_exp_f32 v177, v177\n"
    "v_exp_f32 v178, v178\n"
    "v_mfma_f32_32x32x16_bf16 v[144:159], v[84:87], %7, v[144:159]\n"
    "v_exp_f32 v179, v179\n"
    "v_exp_f32 v180, v180\n"
    "v_exp_f32 v181, v181\n"
    "v_smfmac_f32_32x32x32_bf16 v[208:223], v[92:95], v[168:175], v63\n"
    "v_exp_f32 v182, v182\n"
    "v_exp_f32 v183, v183\n"
    "v_exp_f32 v184, v184\n"
    "v_exp_f32 v185, v185\n"
    "v_smfmac_f32_32x32x32_bf16 v[224:239], v[88:91], v[176:183], v63\n"
    "v_exp_f32 v186, v186\n"
    "v_exp_f32 v187, v187\n"
    "v_exp_f32 v188, v188\n"
    "v_exp_f32 v189, v189\n"
    "v_exp_f32 v190, v190\n"
    "v_exp_f32 v191, v191\n"
    "s_waitcnt vmcnt(12)\n"
    "v_mfma_f32_32x32x16_bf16 v[160:175], v[96:99], %0, 0\n"
    "v_exp_f32 v144, v144\n"
    "v_exp_f32 v145, v145\n"
    "v_exp_f32 v146, v146\n"
    "v_mfma_f32_32x32x16_bf16 v[160:175], v[100:103], %1, v[160:175]\n"
    "v_exp_f32 v147, v147\n"
    "v_exp_f32 v148, v148\n"
    "v_exp_f32 v149, v149\n"
    "v_smfmac_f32_32x32x32_bf16 v[224:239], v[92:95], v[184:191], v63\n"
    "v_exp_f32 v150, v150\n"
    "v_exp_f32 v151, v151\n"
    "v_exp_f32 v152, v152\n"
    "v_exp_f32 v153, v153\n"
    "v_smfmac_f32_32x32x32_bf16 v[240:255], v[88:91], v[144:151], v63\n"
    "v_exp_f32 v154, v154\n"
    "v_exp_f32 v155, v155\n"
    "v_exp_f32 v156, v156\n"
    "v_exp_f32 v157, v157\n"
    "v_exp_f32 v158, v158\n"
    "v_exp_f32 v159, v159\n"
    "v_mfma_f32_32x32x16_bf16 v[176:191], v[96:99], %2, 0\n"
    "buffer_load_dwordx4 v[80:83], %13, %11, s71 offen offset:0 sc1\n"
    "v_exp_f32 v160, v160\n"
    "v_exp_f32 v161, v161\n"
    "v_exp_f32 v162, v162\n"
    "v_mfma_f32_32x32x16_bf16 v[176:191], v[100:103], %3, v[176:191]\n"
    "buffer_load_dwordx4 v[84:87], %13, %11, s71 offen offset:1024 sc1\n"
    "v_exp_f32 v163, v163\n"
    "v_exp_f32 v164, v164\n"
    "v_exp_f32 v165, v165\n"
    "v_smfmac_f32_32x32x32_bf16 v[240:255], v[92:95], v[152:159], v63\n"
    "buffer_load_dwordx4 v[88:91], %13, %11, s71 offen offset:2048 sc1\n"
    "v_exp_f32 v166, v166\n"
    "v_exp_f32 v167, v167\n"
    "v_exp_f32 v168, v168\n"
    "v_exp_f32 v169, v169\n"
    "v_smfmac_f32_32x32x32_bf16 v[192:207], v[104:107], v[160:167], v63\n"
    "buffer_load_dwordx4 v[92:95], %13, %11, s71 offen offset:3072 sc1\n"
    "v_exp_f32 v170, v170\n"
    "v_exp_f32 v171, v171\n"
    "v_exp_f32 v172, v172\n"
    "v_exp_f32 v173, v173\n"
    "v_exp_f32 v174, v174\n"
    "v_exp_f32 v175, v175\n"
    "v_mfma_f32_32x32x16_bf16 v[144:159], v[96:99], %4, 0\n"
    "v_exp_f32 v176, v176\n"
    "v_exp_f32 v177, v177\n"
    "v_exp_f32 v178, v178\n"
    "v_mfma_f32_32x32x16_bf16 v[144:159], v[100:103], %5, v[144:159]\n"
    "v_exp_f32 v179, v179\n"
    "v_exp_f32 v180, v180\n"
    "v_exp_f32 v181, v181\n"
    "v_smfmac_f32_32x32x32_bf16 v[192:207], v[108:111], v[168:175], v63\n"
    "v_exp_f32 v182, v182\n"
    "v_exp_f32 v183, v183\n"
    "v_exp_f32 v184, v184\n"
    "v_exp_f32 v185, v185\n"
    "v_smfmac_f32_32x32x32_bf16 v[208:223], v[104:107], v[176:183], v63\n"
    "v_exp_f32 v186, v186\n"
    "v_exp_f32 v187, v187\n"
    "v_exp_f32 v188, v188\n"
    "v_exp_f32 v189, v189\n"
    "v_exp_f32 v190, v190\n"
    "v_exp_f32 v191, v191\n"
    "v_mfma_f32_32x32x16_bf16 v[160:175], v[96:99], %6, 0\n"
    "v_exp_f32 v144, v144\n"
    "v_exp_f32 v145, v145\n"
    "v_exp_f32 v146, v146\n"
    "v_mfma_f32_32x32x16_bf16 v[160:175], v[100:103], %7, v[160:175]\n"
    "v_exp_f32 v147, v147\n"
    "v_exp_f32 v148, v148\n"
    "v_exp_f32 v149, v149\n"
    "v_smfmac_f32_32x32x32_bf16 v[208:223], v[108:111], v[184:191], v63\n"
    "v_exp_f32 v150, v150\n"
    "v_exp_f32 v151, v151\n"
    "v_exp_f32 v152, v152\n"
    "v_exp_f32 v153, v153\n"
    "v_smfmac_f32_32x32x32_bf16 v[224:239], v[104:107], v[144:151], v63\n"
    "v_exp_f32 v154, v154\n"
    "v_exp_f32 v155, v155\n"
    "v_exp_f32 v156, v156\n"
    "v_exp_f32 v157, v157\n"
    "v_exp_f32 v158, v158\n"
    "v_exp_f32 v159, v159\n"
    "s_waitcnt vmcnt(12)\n"
    "v_mfma_f32_32x32x16_bf16 v[176:191], v[112:115], %0, 0\n"
    "v_exp_f32 v160, v160\n"
    "v_exp_f32 v161, v161\n"
    "v_exp_f32 v162, v162\n"
    "v_mfma_f32_32x32x16_bf16 v[176:191], v[116:119], %1, v[176:191]\n"
    "v_exp_f32 v163, v163\n"
    "v_exp_f32 v164, v164\n"
    "v_exp_f32 v165, v165\n"
    "v_smfmac_f32_32x32x32_bf16 v[224:239], v[108:111], v[152:159], v63\n"
    "v_exp_f32 v166, v166\n"
    "v_exp_f32 v167, v167\n"
    "v_exp_f32 v168, v168\n"
    "v_exp_f32 v169, v169\n"
    "v_smfmac_f32_32x32x32_bf16 v[240:255], v[104:107], v[160:167], v63\n"
    "v_exp_f32 v170, v170\n"
    "v_exp_f32 v171, v171\n"
    "v_exp_f32 v172, v172\n"
    "v_exp_f32 v173, v173\n"
    "v_exp_f32 v174, v174\n"
    "v_exp_f32 v175, v175\n"
    "v_mfma_f32_32x32x16_bf16 v[144:159], v[112:115], %2, 0\n"
    "buffer_load_dwordx4 v[96:99], %13, %11, s72 offen offset:0 sc1\n"
    "v_exp_f32 v176, v176\n"
    "v_exp_f32 v177, v177\n"
    "v_exp_f32 v178, v178\n"
    "v_mfma_f32_32x32x16_bf16 v[144:159], v[116:119], %3, v[144:159]\n"
    "buffer_load_dwordx4 v[100:103], %13, %11, s72 offen offset:1024 sc1\n"
    "v_exp_f32 v179, v179\n"
    "v_exp_f32 v180, v180\n"
    "v_exp_f32 v181, v181\n"
    "v_smfmac_f32_32x32x32_bf16 v[240:255], v[108:111], v[168:175], v63\n"
    "buffer_load_dwordx4 v[104:107], %13, %11, s72 offen offset:2048 sc1\n"
    "v_exp_f32 v182, v182\n"
    "v_exp_f32 v183, v183\n"
    "v_exp_f32 v184, v184\n"
    "v_exp_f32 v185, v185\n"
    "v_smfmac_f32_32x32x32_bf16 v[192:207], v[120:123], v[176:183], v63\n"
    "buffer_load_dwordx4 v[108:111], %13, %11, s72 offen offset:3072 sc1\n"
    "v_exp_f32 v186, v186\n"
    "v_exp_f32 v187, v187\n"
    "v_exp_f32 v188, v188\n"
    "v_exp_f32 v189, v189\n"
    "v_exp_f32 v190, v190\n"
    "v_exp_f32 v191, v191\n"
    "v_mfma_f32_32x32x16_bf16 v[160:175], v[112:115], %4, 0\n"
    "v_exp_f32 v144, v144\n"
    "v_exp_f32 v145, v145\n"
    "v_exp_f32 v146, v146\n"
    "v_mfma_f32_32x32x16_bf16 v[160:175], v[116:119], %5, v[160:175]\n"
    "v_exp_f32 v147, v147\n"
    "v_exp_f32 v148, v148\n"
    "v_exp_f32 v149, v149\n"
    "v_smfmac_f32_32x32x32_bf16 v[192:207], v[124:127], v[184:191], v63\n"
    "v_exp_f32 v150, v150\n"
    "v_exp_f32 v151, v151\n"
    "v_exp_f32 v152, v152\n"
    "v_exp_f32 v153, v153\n"
    "v_smfmac_f32_32x32x32_bf16 v[208:223], v[120:123], v[144:151], v63\n"
    "v_exp_f32 v154, v154\n"
    "v_exp_f32 v155, v155\n"
    "v_exp_f32 v156, v156\n"
    "v_exp_f32 v157, v157\n"
    "v_exp_f32 v158, v158\n"
    "v_exp_f32 v159, v159\n"
    "v_mfma_f32_32x32x16_bf16 v[176:191], v[112:115], %6, 0\n"
    "v_exp_f32 v160, v160\n"
    "v_exp_f32 v161, v161\n"
    "v_exp_f32 v162, v162\n"
    "v_mfma_f32_32x32x16_bf16 v[176:191], v[116:119], %7, v[176:191]\n"
    "v_exp_f32 v163, v163\n"
    "v_exp_f32 v164, v164\n"
    "v_exp_f32 v165, v165\n"
    "v_smfmac_f32_32x32x32_bf16 v[208:223], v[124:127], v[152:159], v63\n"
    "v_exp_f32 v166, v166\n"
    "v_exp_f32 v167, v167\n"
    "v_exp_f32 v168, v168\n"
    "v_exp_f32 v169, v169\n"
    "v_smfmac_f32_32x32x32_bf16 v[224:239], v[120:123], v[160:167], v63\n"
    "v_exp_f32 v170, v170\n"
    "v_exp_f32 v171, v171\n"
    "v_exp_f32 v172, v172\n"
    "v_exp_f32 v173, v173\n"
    "v_exp_f32 v174, v174\n"
    "v_exp_f32 v175, v175\n"
    "s_waitcnt vmcnt(12)\n"
    "v_mfma_f32_32x32x16_bf16 v[144:159], v[128:131], %0, 0\n"
    "v_exp_f32 v176, v176\n"
    "v_exp_f32 v177, v177\n"
    "v_exp_f32 v178, v178\n"
    "v_mfma_f32_32x32x16_bf16 v[144:159], v[132:135], %1, v[144:159]\n"
    "v_exp_f32 v179, v179\n"
    "v_exp_f32 v180, v180\n"
    "v_exp_f32 v181, v181\n"
    "v_smfmac_f32_32x32x32_bf16 v[224:239], v[124:127], v[168:175], v63\n"
    "v_exp_f32 v182, v182\n"
    "v_exp_f32 v183, v183\n"
    "v_exp_f32 v184, v184\n"
    "v_exp_f32 v185, v185\n"
    "v_smfmac_f32_32x32x32_bf16 v[240:255], v[120:123], v[176:183], v63\n"
    "v_exp_f32 v186, v186\n"
    "v_exp_f32 v187, v187\n"
    "v_exp_f32 v188, v188\n"
    "v_exp_f32 v189, v189\n"
    "v_exp_f32 v190, v190\n"
    "v_exp_f32 v191, v191\n"
    "v_mfma_f32_32x32x16_bf16 v[160:175], v[128:131], %2, 0\n"
    "buffer_load_dwordx4 v[112:115], %13, %11, s73 offen offset:0 sc1\n"
    "v_exp_f32 v144, v144\n"
    "v_exp_f32 v145, v145\n"
    "v_exp_f32 v146, v146\n"
    "v_mfma_f32_32x32x16_bf16 v[160:175], v[132:135], %3, v[160:175]\n"
    "buffer_load_dwordx4 v[116:119], %13, %11, s73 offen offset:1024 sc1\n"
    "v_exp_f32 v147, v147\n"
    "v_exp_f32 v148, v148\n"
    "v_exp_f32 v149, v149\n"
    "v_smfmac_f32_32x32x32_bf16 v[240:255], v[124:127], v[184:191], v63\n"
    "buffer_load_dwordx4 v[120:123], %13, %11, s73 offen offset:2048 sc1\n"
    "v_exp_f32 v150, v150\n"
    "v_exp_f32 v151, v151\n"
    "v_exp_f32 v152, v152\n"
    "v_exp_f32 v153, v153\n"
    "v_smfmac_f32_32x32x32_bf16 v[192:207], v[136:139], v[144:151], v63\n"
    "buffer_load_dwordx4 v[124:127], %13, %11, s73 offen offset:3072 sc1\n"
    "v_exp_f32 v154, v154\n"
    "v_exp_f32 v155, v155\n"
    "v_exp_f32 v156, v156\n"
    "v_exp_f32 v157, v157\n"
    "v_exp_f32 v158, v158\n"
    "v_exp_f32 v159, v159\n"
    "v_mfma_f32_32x32x16_bf16 v[176:191], v[128:131], %4, 0\n"
    "v_exp_f32 v160, v160\n"
    "v_exp_f32 v161, v161\n"
    "v_exp_f32 v162, v162\n"
    "v_mfma_f32_32x32x16_bf16 v[176:191], v[132:135], %5, v[176:191]\n"
    "v_exp_f32 v163, v163\n"
    "v_exp_f32 v164, v164\n"
    "v_exp_f32 v165, v165\n"
    "v_smfmac_f32_32x32x32_bf16 v[192:207], v[140:143], v[152:159], v63\n"
    "v_exp_f32 v166, v166\n"
    "v_exp_f32 v167, v167\n"
    "v_exp_f32 v168, v168\n"
    "v_exp_f32 v169, v169\n"
    "v_smfmac_f32_32x32x32_bf16 v[208:223], v[136:139], v[160:167], v63\n"
    "v_exp_f32 v170, v170\n"
    "v_exp_f32 v171, v171\n"
    "v_exp_f32 v172, v172\n"
    "v_exp_f32 v173, v173\n"
    "v_exp_f32 v174, v174\n"
    "v_exp_f32 v175, v175\n"
    "v_mfma_f32_32x32x16_bf16 v[144:159], v[128:131], %6, 0\n"
    "v_exp_f32 v176, v176\n"
    "v_exp_f32 v177, v177\n"
    "v_exp_f32 v178, v178\n"
    "v_mfma_f32_32x32x16_bf16 v[144:159], v[132:135], %7, v[144:159]\n"
    "v_exp_f32 v179, v179\n"
    "v_exp_f32 v180, v180\n"
    "v_exp_f32 v181, v181\n"
    "v_smfmac_f32_32x32x32_bf16 v[208:223], v[140:143], v[168:175], v63\n"
    "v_exp_f32 v182, v182\n"
    "v_exp_f32 v183, v183\n"
    "v_exp_f32 v184, v184\n"
    "v_exp_f32 v185, v185\n"
    "v_smfmac_f32_32x32x32_bf16 v[224:239], v[136:139], v[176:183], v63\n"
    "v_exp_f32 v186, v186\n"
    "v_exp_f32 v187, v187\n"
    "v_exp_f32 v188, v188\n"
    "v_exp_f32 v189, v189\n"
    "v_exp_f32 v190, v190\n"
    "v_exp_f32 v191, v191\n"
    "s_waitcnt vmcnt(12)\n"
    "v_mfma_f32_32x32x16_bf16 v[160:175], v[64:67], %0, 0\n"
    "v_exp_f32 v144, v144\n"
    "v_exp_f32 v145, v145\n"
    "v_exp_f32 v146, v146\n"
    "v_mfma_f32_32x32x16_bf16 v[160:175], v[68:71], %1, v[160:175]\n"
    "v_exp_f32 v147, v147\n"
    "v_exp_f32 v148, v148\n"
    "v_exp_f32 v149, v149\n"
    "v_smfmac_f32_32x32x32_bf16 v[224:239], v[140:143], v[184:191], v63\n"
    "v_exp_f32 v150, v150\n"
    "v_exp_f32 v151, v151\n"
    "v_exp_f32 v152, v152\n"
    "v_exp_f32 v153, v153\n"
    "v_smfmac_f32_32x32x32_bf16 v[240:255], v[136:139], v[144:151], v63\n"
    "v_exp_f32 v154, v154\n"
    "v_exp_f32 v155, v155\n"
    "v_exp_f32 v156, v156\n"
    "v_exp_f32 v157, v157\n"
    "v_exp_f32 v158, v158\n"
    "v_exp_f32 v159, v159\n"
    "v_mfma_f32_32x32x16_bf16 v[176:191], v[64:67], %2, 0\n"
    "buffer_load_dwordx4 v[128:131], %13, %11, s74 offen offset:0 sc1\n"
    "v_exp_f32 v160, v160\n"
    "v_exp_f32 v161, v161\n"
    "v_exp_f32 v162, v162\n"
    "v_mfma_f32_32x32x16_bf16 v[176:191], v[68:71], %3, v[176:191]\n"
    "buffer_load_dwordx4 v[132:135], %13, %11, s74 offen offset:1024 sc1\n"
    "v_exp_f32 v163, v163\n"
    "v_exp_f32 v164, v164\n"
    "v_exp_f32 v165, v165\n"
    "v_smfmac_f32_32x32x32_bf16 v[240:255], v[140:143], v[152:159], v63\n"
    "buffer_load_dwordx4 v[136:139], %13, %11, s74 offen offset:2048 sc1\n"
    "v_exp_f32 v166, v166\n"
    "v_exp_f32 v167, v167\n"
    "v_exp_f32 v168, v168\n"
    "v_exp_f32 v169, v169\n"
    "v_smfmac_f32_32x32x32_bf16 v[192:207], v[72:75], v[160:167], v63\n"
    "buffer_load_dwordx4 v[140:143], %13, %11, s74 offen offset:3072 sc1\n"
    "v_exp_f32 v170, v170\n"
    "v_exp_f32 v171, v171\n"
    "v_exp_f32 v172, v172\n"
    "v_exp_f32 v173, v173\n"
    "v_exp_f32 v174, v174\n"
    "v_exp_f32 v175, v175\n"
    "v_mfma_f32_32x32x16_bf16 v[144:159], v[64:67], %4, 0\n"
    "v_exp_f32 v176, v176\n"
    "v_exp_f32 v177, v177\n"
    "v_exp_f32 v178, v178\n"
    "v_mfma_f32_32x32x16_bf16 v[144:159], v[68:71], %5, v[144:159]\n"
    "v_exp_f32 v179, v179\n"
    "v_exp_f32 v180, v180\n"
    "v_exp_f32 v181, v181\n"
    "v_smfmac_f32_32x32x32_bf16 v[192:207], v[76:79], v[168:175], v63\n"
    "v_exp_f32 v182, v182\n"
    "v_exp_f32 v183, v183\n"
    "v_exp_f32 v184, v184\n"
    "v_exp_f32 v185, v185\n"
    "v_smfmac_f32_32x32x32_bf16 v[208:223], v[72:75], v[176:183], v63\n"
    "v_exp_f32 v186, v186\n"
    "v_exp_f32 v187, v187\n"
    "v_exp_f32 v188, v188\n"
    "v_exp_f32 v189, v189\n"
    "v_exp_f32 v190, v190\n"
    "v_exp_f32 v191, v191\n"
    "v_mfma_f32_32x32x16_bf16 v[160:175], v[64:67], %6, 0\n"
    "v_exp_f32 v144, v144\n"
    "v_exp_f32 v145, v145\n"
    "v_exp_f32 v146, v146\n"
    "v_mfma_f32_32x32x16_bf16 v[160:175], v[68:71], %7, v[160:175]\n"
    "v_exp_f32 v147, v147\n"
    "v_exp_f32 v148, v148\n"
    "v_exp_f32 v149, v149\n"
    "v_smfmac_f32_32x32x32_bf16 v[208:223], v[76:79], v[184:191], v63\n"
    "v_exp_f32 v150, v150\n"
    "v_exp_f32 v151, v151\n"
    "v_exp_f32 v152, v152\n"
    "v_exp_f32 v153, v153\n"
    "v_smfmac_f32_32x32x32_bf16 v[224:239], v[72:75], v[144:151], v63\n"
    "v_exp_f32 v154, v154\n"
    "v_exp_f32 v155, v155\n"
    "v_exp_f32 v156, v156\n"
    "v_exp_f32 v157, v157\n"
    "v_exp_f32 v158, v158\n"
    "v_exp_f32 v159, v159\n"
    "s_waitcnt vmcnt(12)\n"
    "v_mfma_f32_32x32x16_bf16 v[176:191], v[80:83], %0, 0\n"
    "v_exp_f32 v160, v160\n"
    "v_exp_f32 v161, v161\n"
    "v_exp_f32 v162, v162\n"
    "v_mfma_f32_32x32x16_bf16 v[176:191], v[84:87], %1, v[176:191]\n"
    "v_exp_f32 v163, v163\n"
    "v_exp_f32 v164, v164\n"
    "v_exp_f32 v165, v165\n"
    "v_smfmac_f32_32x32x32_bf16 v[224:239], v[76:79], v[152:159], v63\n"
    "v_exp_f32 v166, v166\n"
    "v_exp_f32 v167, v167\n"
    "v_exp_f32 v168, v168\n"
    "v_exp_f32 v169, v169\n"
    "v_smfmac_f32_32x32x32_bf16 v[240:255], v[72:75], v[160:167], v63\n"
    "v_exp_f32 v170, v170\n"
    "v_exp_f32 v171, v171\n"
    "v_exp_f32 v172, v172\n"
    "v_exp_f32 v173, v173\n"
    "v_exp_f32 v174, v174\n"
    "v_exp_f32 v175, v175\n"
    "v_mfma_f32_32x32x16_bf16 v[144:159], v[80:83], %2, 0\n"
    "buffer_load_dwordx4 v[64:67], %13, %11, s75 offen offset:0 sc1\n"
    "v_exp_f32 v176, v176\n"
    "v_exp_f32 v177, v177\n"
    "v_exp_f32 v178, v178\n"
    "v_mfma_f32_32x32x16_bf16 v[144:159], v[84:87], %3, v[144:159]\n"
    "buffer_load_dwordx4 v[68:71], %13, %11, s75 offen offset:1024 sc1\n"
    "v_exp_f32 v179, v179\n"
    "v_exp_f32 v180, v180\n"
    "v_exp_f32 v181, v181\n"
    "v_smfmac_f32_32x32x32_bf16 v[240:255], v[76:79], v[168:175], v63\n"
    "buffer_load_dwordx4 v[72:75], %13, %11, s75 offen offset:2048 sc1\n"
    "v_exp_f32 v182, v182\n"
    "v_exp_f32 v183, v183\n"
    "v_exp_f32 v184, v184\n"
    "v_exp_f32 v185, v185\n"
    "v_smfmac_f32_32x32x32_bf16 v[192:207], v[88:91], v[176:183], v63\n"
    "buffer_load_dwordx4 v[76:79], %13, %11, s75 offen offset:3072 sc1\n"
    "v_exp_f32 v186, v186\n"
    "v_exp_f32 v187, v187\n"
    "v_exp_f32 v188, v188\n"
    "v_exp_f32 v189, v189\n"
    "v_exp_f32 v190, v190\n"
    "v_exp_f32 v191, v191\n"
    "v_mfma_f32_32x32x16_bf16 v[160:175], v[80:83], %4, 0\n"
    "v_exp_f32 v144, v144\n"
    "v_exp_f32 v145, v145\n"
    "v_exp_f32 v146, v146\n"
    "v_mfma_f32_32x32x16_bf16 v[160:175], v[84:87], %5, v[160:175]\n"
    "v_exp_f32 v147, v147\n"
    "v_exp_f32 v148, v148\n"
    "v_exp_f32 v149, v149\n"
    "v_smfmac_f32_32x32x32_bf16 v[192:207], v[92:95], v[184:191], v63\n"
    "v_exp_f32 v150, v150\n"
    "v_exp_f32 v151, v151\n"
    "v_exp_f32 v152, v152\n"
    "v_exp_f32 v153, v153\n"
    "v_smfmac_f32_32x32x32_bf16 v[208:223], v[88:91], v[144:151], v63\n"
    "v_exp_f32 v154, v154\n"
    "v_exp_f32 v155, v155\n"
    "v_exp_f32 v156, v156\n"
    "v_exp_f32 v157, v157\n"
    "v_exp_f32 v158, v158\n"
    "v_exp_f32 v159, v159\n"
    "v_mfma_f32_32x32x16_bf16 v[176:191], v[80:83], %6, 0\n"
    "v_exp_f32 v160, v160\n"
    "v_exp_f32 v161, v161\n"
    "v_exp_f32 v162, v162\n"
    "v_mfma_f32_32x32x16_bf16 v[176:191], v[84:87], %7, v[176:191]\n"
    "v_exp_f32 v163, v163\n"
    "v_exp_f32 v164, v164\n"
    "v_exp_f32 v165, v165\n"
    "v_smfmac_f32_32x32x32_bf16 v[208:223], v[92:95], v[152:159], v63\n"
    "v_exp_f32 v166, v166\n"
    "v_exp_f32 v167, v167\n"
    "v_exp_f32 v168, v168\n"
    "v_exp_f32 v169, v169\n"
    "v_smfmac_f32_32x32x32_bf16 v[224:239], v[88:91], v[160:167], v63\n"
    "v_exp_f32 v170, v170\n"
    "v_exp_f32 v171, v171\n"
    "v_exp_f32 v172, v172\n"
    "v_exp_f32 v173, v173\n"
    "v_exp_f32 v174, v174\n"
    "v_exp_f32 v175, v175\n"
    "s_waitcnt vmcnt(12)\n"
    "v_mfma_f32_32x32x16_bf16 v[144:159], v[96:99], %0, 0\n"
    "v_exp_f32 v176, v176\n"
    "v_exp_f32 v177, v177\n"
    "v_exp_f32 v178, v178\n"
    "v_mfma_f32_32x32x16_bf16 v[144:159], v[100:103], %1, v[144:159]\n"
    "v_exp_f32 v179, v179\n"
    "v_exp_f32 v180, v180\n"
    "v_exp_f32 v181, v181\n"
    "v_smfmac_f32_32x32x32_bf16 v[224:239], v[92:95], v[168:175], v63\n"
    "v_exp_f32 v182, v182\n"
    "v_exp_f32 v183, v183\n"
    "v_exp_f32 v184, v184\n"
    "v_exp_f32 v185, v185\n"
    "v_smfmac_f32_32x32x32_bf16 v[240:255], v[88:91], v[176:183], v63\n"
    "v_exp_f32 v186, v186\n"
    "v_exp_f32 v187, v187\n"
    "v_exp_f32 v188, v188\n"
    "v_exp_f32 v189, v189\n"
    "v_exp_f32 v190, v190\n"
    "v_exp_f32 v191, v191\n"
    "s_waitcnt vmcnt(8)\n"
    "v_mfma_f32_32x32x16_bf16 v[160:175], v[112:115], %0, 0\n"
    "v_exp_f32 v144, v144\n"
    "v_exp_f32 v145, v145\n"
    "v_exp_f32 v146, v146\n"
    "v_mfma_f32_32x32x16_bf16 v[160:175], v[116:119], %1, v[160:175]\n"
    "v_exp_f32 v147, v147\n"
    "v_exp_f32 v148, v148\n"
    "v_exp_f32 v149, v149\n"
    "v_smfmac_f32_32x32x32_bf16 v[240:255], v[92:95], v[184:191], v63\n"
    "v_exp_f32 v150, v150\n"
    "v_exp_f32 v151, v151\n"
    "v_exp_f32 v152, v152\n"
    "v_exp_f32 v153, v153\n"
    "v_smfmac_f32_32x32x32_bf16 v[192:207], v[104:107], v[144:151], v63\n"
    "v_exp_f32 v154, v154\n"
    "v_exp_f32 v155, v155\n"
    "v_exp_f32 v156, v156\n"
    "v_exp_f32 v157, v157\n"
    "v_exp_f32 v158, v158\n"
    "v_exp_f32 v159, v159\n"
    "s_waitcnt vmcnt(4)\n"
    "v_mfma_f32_32x32x16_bf16 v[176:191], v[128:131], %0, 0\n"
    "v_exp_f32 v160, v160\n"
    "v_exp_f32 v161, v161\n"
    "v_exp_f32 v162, v162\n"
    "v_mfma_f32_32x32x16_bf16 v[176:191], v[132:135], %1, v[176:191]\n"
    "v_exp_f32 v163, v163\n"
    "v_exp_f32 v164, v164\n"
    "v_exp_f32 v165, v165\n"
    "v_smfmac_f32_32x32x32_bf16 v[192:207], v[108:111], v[152:159], v63\n"
    "v_exp_f32 v166, v166\n"
    "v_exp_f32 v167, v167\n"
    "v_exp_f32 v168, v168\n"
    "v_exp_f32 v169, v169\n"
    "v_smfmac_f32_32x32x32_bf16 v[192:207], v[120:123], v[160:167], v63\n"
    "v_exp_f32 v170, v170\n"
    "v_exp_f32 v171, v171\n"
    "v_exp_f32 v172, v172\n"
    "v_exp_f32 v173, v173\n"
    "v_exp_f32 v174, v174\n"
    "v_exp_f32 v175, v175\n"
    "s_waitcnt vmcnt(0)\n"
    "v_mfma_f32_32x32x16_bf16 v[144:159], v[64:67], %0, 0\n"
    "v_exp_f32 v176, v176\n"
    "v_exp_f32 v177, v177\n"
    "v_exp_f32 v178, v178\n"
    "v_mfma_f32_32x32x16_bf16 v[144:159], v[68:71], %1, v[144:159]\n"
    "v_exp_f32 v179, v179\n"
    "v_exp_f32 v180, v180\n"
    "v_exp_f32 v181, v181\n"
    "v_smfmac_f32_32x32x32_bf16 v[192:207], v[124:127], v[168:175], v63\n"
    "v_exp_f32 v182, v182\n"
    "v_exp_f32 v183, v183\n"
    "v_exp_f32 v184, v184\n"
    "v_exp_f32 v185, v185\n"
    "v_smfmac_f32_32x32x32_bf16 v[192:207], v[136:139], v[176:183], v63\n"
    "v_exp_f32 v186, v186\n"
    "v_exp_f32 v187, v187\n"
    "v_exp_f32 v188, v188\n"
    "v_exp_f32 v189, v189\n"
    "v_exp_f32 v190, v190\n"
    "v_exp_f32 v191, v191\n"
    "v_mfma_f32_32x32x16_bf16 v[160:175], v[96:99], %2, 0\n"
    "v_exp_f32 v144, v144\n"
    "v_exp_f32 v145, v145\n"
    "v_exp_f32 v146, v146\n"
    "v_mfma_f32_32x32x16_bf16 v[160:175], v[100:103], %3, v[160:175]\n"
    "v_exp_f32 v147, v147\n"
    "v_exp_f32 v148, v148\n"
    "v_exp_f32 v149, v149\n"
    "v_smfmac_f32_32x32x32_bf16 v[192:207], v[140:143], v[184:191], v63\n"
    "v_exp_f32 v150, v150\n"
    "v_exp_f32 v151, v151\n"
    "v_exp_f32 v152, v152\n"
    "v_exp_f32 v153, v153\n"
    "v_smfmac_f32_32x32x32_bf16 v[192:207], v[72:75], v[144:151], v63\n"
    "v_exp_f32 v154, v154\n"
    "v_exp_f32 v155, v155\n"
    "v_exp_f32 v156, v156\n"
    "v_exp_f32 v157, v157\n"
    "v_exp_f32 v158, v158\n"
    "v_exp_f32 v159, v159\n"
    "s_mov_b32 s58, 0x100000\n"
    "Lwr_%=:\n"
    "ds_read_b32 v62, %21 offset:56\n"
    "s_waitcnt lgkmcnt(0)\n"
    "v_readfirstlane_b32 s59, v62\n"
    "s_cmp_ge_u32 s59, 4\n"
    "s_cbranch_scc1 Lwrx_%=\n"
    "s_sleep 1\n"
    "s_sub_u32 s58, s58, 1\n"
    "s_cbranch_scc0 Lwr_%=\n"
    "Lwrx_%=:\n"
    "v_mfma_f32_32x32x16_bf16 v[176:191], v[112:115], %2, 0\n"
    "v_exp_f32 v160, v160\n"
    "v_exp_f32 v161, v161\n"
    "v_exp_f32 v162, v162\n"
    "v_mfma_f32_32x32x16_bf16 v[176:191], v[116:119], %3, v[176:191]\n"
    "v_exp_f32 v163, v163\n"
    "v_exp_f32 v164, v164\n"
    "v_exp_f32 v165, v165\n"
    "v_smfmac_f32_32x32x32_bf16 v[192:207], v[76:79], v[152:159], v63\n"
    "v_exp_f32 v166, v166\n"
    "v_exp_f32 v167, v167\n"
    "v_exp_f32 v168, v168\n"
    "v_exp_f32 v169, v169\n"
    "v_smfmac_f32_32x32x32_bf16 v[208:223], v[104:107], v[160:167], v63\n"
    "v_exp_f32 v170, v170\n"
    "v_exp_f32 v171, v171\n"
    "v_exp_f32 v172, v172\n"
    "v_exp_f32 v173, v173\n"
    "v_exp_f32 v174, v174\n"
    "v_exp_f32 v175, v175\n"
    "v_mfma_f32_32x32x16_bf16 v[144:159], v[128:131], %2, 0\n"
    "v_exp_f32 v176, v176\n"
    "ds_write_b128 %15, v[192:195] offset:0\n"
    "v_exp_f32 v177, v177\n"
    "v_exp_f32 v178, v178\n"
    "v_mfma_f32_32x32x16_bf16 v[144:159], v[132:135], %3, v[144:159]\n"
    "ds_write_b128 %15, v[196:199] offset:1024\n"
    "v_exp_f32 v179, v179\n"
    "v_exp_f32 v180, v180\n"
    "v_exp_f32 v181, v181\n"
    "v_smfmac_f32_32x32x32_bf16 v[208:223], v[108:111], v[168:175], v63\n"
    "ds_write_b128 %15, v[200:203] offset:2048\n"
    "v_exp_f32 v182, v182\n"
    "v_exp_f32 v183, v183\n"
    "v_exp_f32 v184, v184\n"
    "v_exp_f32 v185, v185\n"
    "v_smfmac_f32_32x32x32_bf16 v[208:223], v[120:123], v[176:183], v63\n"
    "ds_write_b128 %15, v[204:207] offset:3072\n"
    "v_exp_f32 v186, v186\n"
    "v_exp_f32 v187, v187\n"
    "s_mov_b64 exec, 1\n"
    "ds_add_u32 %21, v61 offset:60\n"
    "s_mov_b64 exec, -1\n"
    "v_exp_f32 v188, v188\n"
    "v_exp_f32 v189, v189\n"
    "v_exp_f32 v190, v190\n"
    "v_exp_f32 v191, v191\n"
    "v_mfma_f32_32x32x16_bf16 v[160:175], v[64:67], %2, 0\n"
    "v_exp_f32 v144, v144\n"
    "v_exp_f32 v145, v145\n"
    "v_exp_f32 v146, v146\n"
    "v_mfma_f32_32x32x16_bf16 v[160:175], v[68:71], %3, v[160:175]\n"
    "v_exp_f32 v147, v147\n"
    "v_exp_f32 v148, v148\n"
    "v_exp_f32 v149, v149\n"
    "v_smfmac_f32_32x32x32_bf16 v[208:223], v[124:127], v[184:191], v63\n"
    "v_exp_f32 v150, v150\n"
    "v_exp_f32 v151, v151\n"
    "v_exp_f32 v152, v152\n"
    "v_exp_f32 v153, v153\n"
    "v_smfmac_f32_32x32x32_bf16 v[208:223], v[136:139], v[144:151], v63\n"
    "v_exp_f32 v154, v154\n"
    "v_exp_f32 v155, v155\n"
    "v_exp_f32 v156, v156\n"
    "v_exp_f32 v157, v157\n"
    "v_exp_f32 v158, v158\n"
    "v_exp_f32 v159, v159\n"
    "v_mfma_f32_32x32x16_bf16 v[176:191], v[96:99], %4, 0\n"
    "v_exp_f32 v160, v160\n"
    "v_exp_f32 v161, v161\n"
    "v_exp_f32 v162, v162\n"
    "v_mfma_f32_32x32x16_bf16 v[176:191], v[100:103], %5, v[176:191]\n"
    "v_exp_f32 v163, v163\n"
    "v_exp_f32 v164, v164\n"
    "v_exp_f32 v165, v165\n"
    "v_smfmac_f32_32x32x32_bf16 v[208:223], v[140:143], v[152:159], v63\n"
    "v_exp_f32 v166, v166\n"
    "v_exp_f32 v167, v167\n"
    "v_exp_f32 v168, v168\n"
    "v_exp_f32 v169, v169\n"
    "v_smfmac_f32_32x32x32_bf16 v[208:223], v[72:75], v[160:167], v63\n"
    "v_exp_f32 v170, v170\n"
    "v_exp_f32 v171, v171\n"
    "v_exp_f32 v172, v172\n"
    "v_exp_f32 v173, v173\n"
    "v_exp_f32 v174, v174\n"
    "v_exp_f32 v175, v175\n"
    "v_mfma_f32_32x32x16_bf16 v[144:159], v[112:115], %4, 0\n"
    "v_exp_f32 v176, v176\n"
    "v_exp_f32 v177, v177\n"
    "v_exp_f32 v178, v178\n"
    "v_mfma_f32_32x32x16_bf16 v[144:159], v[116:119], %5, v[144:159]\n"
    "v_exp_f32 v179, v179\n"
    "v_exp_f32 v180, v180\n"
    "v_exp_f32 v181, v181\n"
    "v_smfmac_f32_32x32x32_bf16 v[208:223], v[76:79], v[168:175], v63\n"
    "v_exp_f32 v182, v182\n"
    "v_exp_f32 v183, v183\n"
    "v_exp_f32 v184, v184\n"
    "v_exp_f32 v185, v185\n"
    "v_smfmac_f32_32x32x32_bf16 v[224:239], v[104:107], v[176:183], v63\n"
    "v_exp_f32 v186, v186\n"
    "v_exp_f32 v187, v187\n"
    "v_exp_f32 v188, v188\n"
    "v_exp_f32 v189, v189\n"
    "v_exp_f32 v190, v190\n"
    "v_exp_f32 v191, v191\n"
    "v_mfma_f32_32x32x16_bf16 v[160:175], v[128:131], %4, 0\n"
    "v_exp_f32 v144, v144\n"
    "ds_write_b128 %15, v[208:211] offset:16384\n"
    "v_exp_f32 v145, v145\n"
    "v_exp_f32 v146, v146\n"
    "v_mfma_f32_32x32x16_bf16 v[160:175], v[132:135], %5, v[160:175]\n"
    "ds_write_b128 %15, v[212:215] offset:17408\n"
    "v_exp_f32 v147, v147\n"
    "v_exp_f32 v148, v148\n"
    "v_exp_f32 v149, v149\n"
    "v_smfmac_f32_32x32x32_bf16 v[224:239], v[108:111], v[184:191], v63\n"
    "ds_write_b128 %15, v[216:219] offset:18432\n"
    "v_exp_f32 v150, v150\n"
    "v_exp_f32 v151, v151\n"
    "v_exp_f32 v152, v152\n"
    "v_exp_f32 v153, v153\n"
    "v_smfmac_f32_32x32x32_bf16 v[224:239], v[120:123], v[144:151], v63\n"
    "ds_write_b128 %15, v[220:223] offset:19456\n"
    "v_exp_f32 v154, v154\n"
    "v_exp_f32 v155, v155\n"
    "s_mov_b64 exec, 1\n"
    "ds_add_u32 %21, v61 offset:64\n"
    "s_mov_b64 exec, -1\n"
    "v_exp_f32 v156, v156\n"
    "v_exp_f32 v157, v157\n"
    "v_exp_f32 v158, v158\n"
    "v_exp_f32 v159, v159\n"
    "v_mfma_f32_32x32x16_bf16 v[176:191], v[64:67], %4, 0\n"
    "v_exp_f32 v160, v160\n"
    "v_exp_f32 v161, v161\n"
    "v_exp_f32 v162, v162\n"
    "v_mfma_f32_32x32x16_bf16 v[176:191], v[68:71], %5, v[176:191]\n"
    "v_exp_f32 v163, v163\n"
    "v_exp_f32 v164, v164\n"
    "v_exp_f32 v165, v165\n"
    "v_smfmac_f32_32x32x32_bf16 v[224:239], v[124:127], v[152:159], v63\n"
    "v_exp_f32 v166, v166\n"
    "v_exp_f32 v167, v167\n"
    "v_exp_f32 v168, v168\n"
    "v_exp_f32 v169, v169\n"
    "v_smfmac_f32_32x32x32_bf16 v[224:239], v[136:139], v[160:167], v63\n"
    "v_exp_f32 v170, v170\n"
    "v_exp_f32 v171, v171\n"
    "v_exp_f32 v172, v172\n"
    "v_exp_f32 v173, v173\n"
    "v_exp_f32 v174, v174\n"
    "v_exp_f32 v175, v175\n"
    "v_mfma_f32_32x32x16_bf16 v[144:159], v[96:99], %6, 0\n"
    "v_exp_f32 v176, v176\n"
    "v_exp_f32 v177, v177\n"
    "v_exp_f32 v178, v178\n"
    "v_mfma_f32_32x32x16_bf16 v[144:159], v[100:103], %7, v[144:159]\n"
    "v_exp_f32 v179, v179\n"
    "v_exp_f32 v180, v180\n"
    "v_exp_f32 v181, v181\n"
    "v_smfmac_f32_32x32x32_bf16 v[224:239], v[140:143], v[168:175], v63\n"
    "v_exp_f32 v182, v182\n"
    "v_exp_f32 v183, v183\n"
    "v_exp_f32 v184, v184\n"
    "v_exp_f32 v185, v185\n"
    "v_smfmac_f32_32x32x32_bf16 v[224:239], v[72:75], v[176:183], v63\n"
    "v_exp_f32 v186, v186\n"
    "v_exp_f32 v187, v187\n"
    "v_exp_f32 v188, v188\n"
    "v_exp_f32 v189, v189\n"
    "v_exp_f32 v190, v190\n"
    "v_exp_f32 v191, v191\n"
    "v_mfma_f32_32x32x16_bf16 v[160:175], v[112:115], %6, 0\n"
    "v_exp_f32 v144, v144\n"
    "v_exp_f32 v145, v145\n"
    "v_exp_f32 v146, v146\n"
    "v_mfma_f32_32x32x16_bf16 v[160:175], v[116:119], %7, v[160:175]\n"
    "v_exp_f32 v147, v147\n"
    "v_exp_f32 v148, v148\n"
    "v_exp_f32 v149, v149\n"
    "v_smfmac_f32_32x32x32_bf16 v[224:239], v[76:79], v[184:191], v63\n"
    "v_exp_f32 v150, v150\n"
    "v_exp_f32 v151, v151\n"
    "v_exp_f32 v152, v152\n"
    "v_exp_f32 v153, v153\n"
    "v_smfmac_f32_32x32x32_bf16 v[240:255], v[104:107], v[144:151], v63\n"
    "v_exp_f32 v154, v154\n"
    "v_exp_f32 v155, v155\n"
    "v_exp_f32 v156, v156\n"
    "v_exp_f32 v157, v157\n"
    "v_exp_f32 v158, v158\n"
    "v_exp_f32 v159, v159\n"
    "v_mfma_f32_32x32x16_bf16 v[176:191], v[128:131], %6, 0\n"
    "v_exp_f32 v160, v160\n"
    "ds_write_b128 %15, v[224:227] offset:32768\n"
    "v_exp_f32 v161, v161\n"
    "v_exp_f32 v162, v162\n"
    "v_mfma_f32_32x32x16_bf16 v[176:191], v[132:135], %7, v[176:191]\n"
    "ds_write_b128 %15, v[228:231] offset:33792\n"
    "v_exp_f32 v163, v163\n"
    "v_exp_f32 v164, v164\n"
    "v_exp_f32 v165, v165\n"
    "v_smfmac_f32_32x32x32_bf16 v[240:255], v[108:111], v[152:159], v63\n"
    "ds_write_b128 %15, v[232:235] offset:34816\n"
    "v_exp_f32 v166, v166\n"
    "v_exp_f32 v167, v167\n"
    "v_exp_f32 v168, v168\n"
    "v_exp_f32 v169, v169\n"
    "v_smfmac_f32_32x32x32_bf16 v[240:255], v[120:123], v[160:167], v63\n"
    "ds_write_b128 %15, v[236:239] offset:35840\n"
    "v_exp_f32 v170, v170\n"
    "v_exp_f32 v171, v171\n"
    "s_mov_b64 exec, 1\n"
    "ds_add_u32 %21, v61 offset:68\n"
    "s_mov_b64 exec, -1\n"
    "v_exp_f32 v172, v172\n"
    "v_exp_f32 v173, v173\n"
    "v_exp_f32 v174, v174\n"
    "v_exp_f32 v175, v175\n"
    "v_mfma_f32_32x32x16_bf16 v[144:159], v[64:67], %6, 0\n"
    "v_exp_f32 v176, v176\n"
    "v_exp_f32 v177, v177\n"
    "v_exp_f32 v178, v178\n"
    "v_mfma_f32_32x32x16_bf16 v[144:159], v[68:71], %7, v[144:159]\n"
    "v_exp_f32 v179, v179\n"
    "v_exp_f32 v180, v180\n"
    "v_exp_f32 v181, v181\n"
    "v_smfmac_f32_32x32x32_bf16 v[240:255], v[124:127], v[168:175], v63\n"
    "v_exp_f32 v182, v182\n"
    "v_exp_f32 v183, v183\n"
    "v_exp_f32 v184, v184\n"
    "v_exp_f32 v185, v185\n"
    "v_smfmac_f32_32x32x32_bf16 v[240:255], v[136:139], v[176:183], v63\n"
    "v_exp_f32 v186, v186\n"
    "v_exp_f32 v187, v187\n"
    "v_exp_f32 v188, v188\n"
    "v_exp_f32 v189, v189\n"
    "v_exp_f32 v190, v190\n"
    "v_exp_f32 v191, v191\n"
    "v_exp_f32 v144, v144\n"
    "v_exp_f32 v145, v145\n"
    "v_exp_f32 v146, v146\n"
    "v_exp_f32 v147, v147\n"
    "v_exp_f32 v148, v148\n"
    "v_exp_f32 v149, v149\n"
    "v_smfmac_f32_32x32x32_bf16 v[240:255], v[140:143], v[184:191], v63\n"
    "v_exp_f32 v150, v150\n"
    "v_exp_f32 v151, v151\n"
    "v_exp_f32 v152, v152\n"
    "v_exp_f32 v153, v153\n"
    "v_smfmac_f32_32x32x32_bf16 v[240:255], v[72:75], v[144:151], v63\n"
    "v_exp_f32 v154, v154\n"
    "v_exp_f32 v155, v155\n"
    "v_exp_f32 v156, v156\n"
    "v_exp_f32 v157, v157\n"
    "v_exp_f32 v158, v158\n"
    "v_exp_f32 v159, v159\n"
    "s_nop 1\n"
    "v_smfmac_f32_32x32x32_bf16 v[240:255], v[76:79], v[152:159], v63\n"
    "s_nop 7\n"
    "s_nop 3\n"
    "ds_write_b128 %15, v[240:243] offset:49152\n"
    "ds_write_b128 %15, v[244:247] offset:50176\n"
    "ds_write_b128 %15, v[248:251] offset:51200\n"
    "ds_write_b128 %15, v[252:255] offset:52224\n"
    "s_mov_b64 exec, 1\n"
    "ds_add_u32 %21, v61 offset:72\n"
    "s_mov_b64 exec, -1\n"
    "s_setprio 0\n"

                    : "+v"(Q0), "+v"(Q1), "+v"(Q2), "+v"(Q3), "+v"(Q4), "+v"(Q5), "+v"(Q6), "+v"(Q7), "+v"(stv), "+v"(wa0), "+v"(wa1)
                    : "s"(rkv), "s"(rws), "v"(a * 65536 + lane * 16), "v"(L_W + lane * 16), "v"(L_W + a * 4096 + lane * 16), "s"(ib >> 1), "s"(pass),
                      "v"(xb), "v"(WS_FLAGS + (16 * a + (lane >> 2)) * 128 + (lane & 3) * 4), "s"(X), "v"(vM), "v"(L_W + a * 2048 + lane * 16)
                    : "memory", "scc", "vcc", "v60", "v61", "v62", "v63", "v64", "v65", "v66", "v67", "v68", "v69", "v70", "v71", "v72", "v73", "v74", "v75", "v76", "v77", "v78", "v79", "v80", "v81", "v82", "v83", "v84", "v85", "v86", "v87", "v88", "v89", "v90", "v91", "v92", "v93", "v94", "v95", "v96", "v97", "v98", "v99", "v100", "v101", "v102", "v103", "v104", "v105", "v106", "v107", "v108", "v109", "v110", "v111", "v112", "v113", "v114", "v115", "v116", "v117", "v118", "v119", "v120", "v121", "v122", "v123", "v124", "v125", "v126", "v127", "v128", "v129", "v130", "v131", "v132", "v133", "v134", "v135", "v136", "v137", "v138", "v139", "v140", "v141", "v142", "v143", "v144", "v145", "v146", "v147", "v148", "v149", "v150", "v151", "v152", "v153", "v154", "v155", "v156", "v157", "v158", "v159", "v160", "v161", "v162", "v163", "v164", "v165", "v166", "v167", "v168", "v169", "v170", "v171", "v172", "v173", "v174", "v175", "v176", "v177", "v178", "v179", "v180", "v181", "v182", "v183", "v184", "v185", "v186", "v187", "v188", "v189", "v190", "v191", "v192", "v193", "v194", "v195", "v196", "v197", "v198", "v199", "v200", "v201", "v202", "v203", "v204", "v205", "v206", "v207", "v208", "v209", "v210", "v211", "v212", "v213", "v214", "v215", "v216", "v217", "v218", "v219", "v220", "v221", "v222", "v223", "v224", "v225", "v226", "v227", "v228", "v229", "v230", "v231", "v232", "v233", "v234", "v235", "v236", "v237", "v238", "v239", "v240", "v241", "v242", "v243", "v244", "v245", "v246", "v247", "v248", "v249", "v250", "v251", "v252", "v253", "v254", "v255", "s56", "s57", "s58", "s59", "s60", "s61", "s62", "s63", "s64", "s65", "s66", "s67", "s68", "s69", "s70", "s71", "s72", "s73", "s74", "s75", "s76", "s77", "s78", "s79");
                STAMP(12);
                if (pass) break;
                WAITC(C_V, 1, 0);
                if (__builtin_expect(__builtin_amdgcn_readfirstlane(ldv<u32>(L_MISC + 4 * C_V)) == 1, 1)) break;
                int ln; asm volatile("v_mbcnt_lo_u32_b32 %0, -1, 0\n\tv_mbcnt_hi_u32_b32 %0, -1, %0" : "=v"(ln));
                float mx[4] = {-INFINITY, -INFINITY, -INFINITY, -INFINITY};
                f32x16 z;
#pragma unroll
                for (int r = 0; r < 16; ++r) z[r] = 0.f;
#pragma unroll 1
                for (int t = 0; t < 16; ++t) {
                    const u32x4 k0 = __builtin_amdgcn_raw_buffer_load_b128(rkv, a * 65536 + ln * 16, t * 4096, SC1), k1 = __builtin_amdgcn_raw_buffer_load_b128(rkv, a * 65536 + ln * 16 + 1024, t * 4096, SC1);
#define MXQ(q, A_, B_) { const f32x16 s_ = mfma32(k1, B_, mfma32(k0, A_, z)); _Pragma("unroll") for (int r = 0; r < 16; ++r) mx[q] = fmaxf(mx[q], s_[r]); }
                    MXQ(0, Q0, Q1) MXQ(1, Q2, Q3) MXQ(2, Q4, Q5) MXQ(3, Q6, Q7)
                }
                const u32 mw = L_W + 65536 + (a * 64 + ln) * 4, mr = L_W + 65536 + (ln & 31) * 4;
#pragma unroll
                for (int q = 0; q < 4; ++q) st<float>(mw + q * 1024, mx[q]);
                SIG(C_MX);
                WAITC(C_MX, 4, 0);
#define FIXQ(q, B_) { float v_ = -INFINITY; _Pragma("unroll") for (int e = 0; e < 8; ++e) v_ = fmaxf(v_, ld<float>(mr + q * 1024 + e * 128)); \
                const float m1 = rbf(v_), m2 = rbf(v_ - m1), m3 = (v_ - m1) - m2; if (ln >= 32) { B_[2] = pk(-m1, -m2); B_[3] = pk(-m3, 0.f); } }
                FIXQ(0, Q1) FIXQ(1, Q3) FIXQ(2, Q5) FIXQ(3, Q7)
            }
        }
        STAMP(13);
        if (a < 3) {
            int ln; asm volatile("v_mbcnt_lo_u32_b32 %0, -1, 0\n\tv_mbcnt_hi_u32_b32 %0, -1, %0" : "=v"(ln));
            const int pm = (m + 1 + a) & 3;
            const u32 src = WS_OX + (g * 4 + pm) * 7168;
            if (has) WAITC(C_CR, 4, 0);
            {
                u32x4 v[6];
                for (u32 i = 0; i < SPIN_CAP; ++i) {
                    u32 mm = 0;
#pragma unroll
                    for (int j = 0; j < 6; ++j) { const int idx = min(64 * j + ln, 335); v[j] = __builtin_amdgcn_raw_buffer_load_b128(rws, src + idx * 16, 0, SC1); }
#pragma unroll
                    for (int j = 0; j < 6; ++j) mm = max(max(mm, max(v[j][0], v[j][1])), max(v[j][2], v[j][3]));
                    if (__all(mm != ~0u)) break;
                    asm volatile("" ::: "memory");
                }
#pragma unroll
                for (int j = 0; j < 6; ++j) { const int idx = min(64 * j + ln, 335), q = idx / 112; st<u32x4>(L_X + (28 * q + 7 * pm) * 256 + (idx - 112 * q) * 16, v[j]); }
                SIG(C_G1);
            }
            STAMP(14);
            {
                u32x4 v[2];
                for (u32 i = 0; i < SPIN_CAP; ++i) {
                    u32 mm = 0;
#pragma unroll
                    for (int j = 0; j < 2; ++j) { const int idx = min(64 * j + ln, 111); v[j] = __builtin_amdgcn_raw_buffer_load_b128(rws, src + 3 * 1792 + idx * 16, 0, SC1); }
#pragma unroll
                    for (int j = 0; j < 2; ++j) mm = max(max(mm, max(v[j][0], v[j][1])), max(v[j][2], v[j][3]));
                    if (__all(mm != ~0u)) break;
                    asm volatile("" ::: "memory");
                }
#pragma unroll
                for (int j = 0; j < 2; ++j) { const int idx = min(64 * j + ln, 111); st<u32x4>(L_X + (84 + 7 * pm) * 256 + idx * 16, v[j]); }
                SIG(C_G2);
            }
            STAMP(15);
        }
        asm volatile("Lpf_end_a:");
    } else {
        LANEVARS();
        const int w = wid - 4, ch = w >> 1, nh = w & 1;
        asm volatile("s_waitcnt vmcnt(0)" : "+v"(pf) :: "memory");
        PRIO(3);
        STAMP(1);
        const u32 vrow = (lane / 14) * 896 + vcol;
        const rsrc_t ro = mkrsrc(wo, 448 * 896);
        u32 X = 0;
        if (has) {
            const u32 slot = L_R2 + w * 4352;
            f32x16 kacc;
#pragma unroll
            for (int r = 0; r < 16; ++r) kacc[r] = 0.f;
            const float dt = rbf(expf(bflo(pdt)));
            const u32 wr = slot + lane * 4, rb = slot + l15 * 272 + h * 16;
            const int rr = (l31 & 3) + 4 * (l31 >> 3);
            const u32 ra = slot + ((l31 & 4) ? (u32)((0xBA98765443222100ull >> (4 * rr)) & 15) : (u32)rr) * 272 + h * 16;
#pragma unroll
            for (int rd = 0; rd < 2; ++rd) {
                const float are = rd ? bfhi(pAr) : bflo(pAr), aim = rd ? bfhi(pAi) : bflo(pAi);
                const f32x2 Bc = {dt * (rd ? bfhi(pBr) : bflo(pBr)), -dt * (rd ? bfhi(pBi) : bflo(pBi))}, Cc = {rd ? bfhi(pCr) : bflo(pCr), -(rd ? bfhi(pCi) : bflo(pCi))};
                const float al = (dt * are) * 1.4426950408889634f, bl = (dt * aim) * 0.15915494309189535f;
                const f32x2 z1 = zpow(al, -bl, 1.f), z8 = zpow(al, bl, 8.f), zw = zpow(al, -bl, 256.f);
                u32x4 Bf[8], Af[8];
#define PUT(row, v) st<u32>(wr + (row) * 272, pk((v).x, (v).y))
                {
                    const f32x2 z16 = cmul(z8, z8), z32 = cmul(z16, z16);
                    f32x2 c[4] = {{1.f, 0.f}, z8, z16, cmul(z8, z16)};
#pragma unroll
                    for (int j = 0; j < 8; ++j) {
#pragma unroll
                        for (int e = 0; e < 4; ++e) { PUT(4 * (j & 3) + e, c[e]); if (j < 7) c[e] = (j | e) ? cmul(c[e], z32) : z32; }
                        if (j == 3) FRAG8(Bf, OUT_W, rb, 0x0000ffff0000ffffull);
                    }
                    FRAG8(Bf, OUT_RW, rb, 0xffff0000ffff0000ull);
                }
                {
                    const f32x2 z2 = cmul(z1, z1), z4 = cmul(z2, z2);
                    f32x2 r[9];
                    r[0] = cmul(Cc, Bc); r[1] = cmul(r[0], z1); r[2] = cmul(r[0], z2); r[3] = cmul(r[1], z2);
#pragma unroll
                    for (int j = 4; j < 9; ++j) r[j] = cmul(r[j - 4], z4);
                    const f32x2 zw2 = cmul(zw, zw);
                    const f32x2 a0 = cmul(r[0], zw), a2 = cmul(r[2], zw), a4 = cmul(r[4], zw), a6 = cmul(r[6], zw), a8 = cmul(r[8], zw);
                    const f32x2 b0 = cmul(r[0], zw2), b4 = cmul(r[4], zw2), b8 = cmul(r[8], zw2), c0 = cmul(a0, zw2), c4 = cmul(a4, zw2), c8 = cmul(a8, zw2);
                    const f32x2 d0 = cmul(b0, zw2), d8 = cmul(b8, zw2), e0 = cmul(c0, zw2), e8 = cmul(c8, zw2), f0 = cmul(d0, zw2), f8 = cmul(d8, zw2), g0 = cmul(e0, zw2), g8 = cmul(e8, zw2);
#pragma unroll
                    for (int j = 0; j < 9; ++j) PUT(j, r[j]);
                    PUT(9, b0); PUT(10, b4); PUT(11, b8); PUT(12, d0); PUT(13, d8); PUT(14, e0); PUT(15, e8);
                    FRAG8(Af, OUT_W, ra, 0x0f0f0f0f0f0f0f0full);
                    PUT(0, a0); PUT(1, a2); PUT(2, a4); PUT(3, a6); PUT(4, a8); PUT(5, c0); PUT(6, c4); PUT(7, c8); PUT(8, f0); PUT(9, f8); PUT(10, g0); PUT(11, g8);
                    FRAG8(Af, OUT_RW, ra, 0xf0f0f0f0f0f0f0f0ull);
                }
                asm volatile("s_waitcnt lgkmcnt(0)" : "+v"(Af[0]), "+v"(Af[1]), "+v"(Af[2]), "+v"(Af[3]), "+v"(Af[4]), "+v"(Af[5]), "+v"(Af[6]), "+v"(Af[7]),
                             "+v"(Bf[0]), "+v"(Bf[1]), "+v"(Bf[2]), "+v"(Bf[3]), "+v"(Bf[4]), "+v"(Bf[5]), "+v"(Bf[6]), "+v"(Bf[7]) :: "memory");
#pragma unroll
                for (int s = 0; s < 8; ++s) kacc = mfma32(Af[s], Bf[s], kacc);
                if (rd == 0) STAMP(2);
            }
#pragma unroll
            for (int r4 = 0; r4 < 4; ++r4) st<f32x4>(slot + r4 * 1024 + lane * 16, (f32x4){kacc[4 * r4], kacc[4 * r4 + 1], kacc[4 * r4 + 2], kacc[4 * r4 + 3]});
            SIG(C_BP);
            STAMP(3);
            WAITC(C_KV, 2, 0);
            X = __builtin_amdgcn_readfirstlane(ld<u32>(L_MISC + 124)) + 1u;
            const u32 fo = WS_FLAGS + lane * 128 + (b / 56) * 4;
            u32 fl = X;
            if (w == 0) asm volatile("buffer_load_dword %0, %1, %2, 0 offen sc1" : "=&v"(fl) : "v"(fo), "s"(rws) : "memory");
            WAITC(C_BP, 4, 0);
            float v[16];
#pragma unroll
            for (int r4 = 0; r4 < 4; ++r4) { const f32x4 t = ld<f32x4>(L_R2 + (w ^ 1) * 4352 + r4 * 1024 + lane * 16);
#pragma unroll
                for (int e = 0; e < 4; ++e) v[4 * r4 + e] = kacc[4 * r4 + e] + t[e]; }
            SIG(C_BR);
            const u32 kb = L_R1 + ch * 8320 + (2040 - 8 * l31) * 2;
            auto put_stair8 = [&](int B, float gg, float nn) { const float mm = (l31 & 1) ? nn : gg; const u32 pg = pk(gg, gg), pn = pk(nn, nn);
                st<u32x4>(kb - B * 512, (u32x4){pn, pk(nn, mm), pg, pg}); st<u32x4>(kb - B * 512 + 4160, (u32x4){pn, pn, pk(mm, gg), pg}); };
            if (nh == 0) {
                if (lane == 0) v[0] += bflo(pds);
                st<u32x4>(kb - h * 512, (u32x4){pk(v[7], v[6]), pk(v[5], v[4]), pk(v[3], v[2]), pk(v[1], v[0])});
                st<u32x4>(kb - h * 512 + 4160, (u32x4){pk(v[8], v[7]), pk(v[6], v[5]), pk(v[4], v[3]), pk(v[2], v[1])});
                put_stair8(5 + 2 * h, v[14], v[15]);
                if (lane < 4) st<u32x4>(L_R1 + ch * 8320 + (lane < 2 ? 4160 : 0) + 4096 + (lane & 1) * 16, (u32x4){lane == 0 ? pk(v[0], 0.f) : 0u, 0u, 0u, 0u});
            } else {
                const u32 p0 = pk(v[9], v[9]), p4 = pk(v[10], v[10]), p8 = pk(v[11], v[11]);
                st<u32x4>(kb - (2 + h) * 512, (u32x4){p8, p4, pk(v[10], v[9]), p0});
                st<u32x4>(kb - (2 + h) * 512 + 4160, (u32x4){p8, pk(v[11], v[10]), p4, p0});
                put_stair8(4 + 2 * h, v[12], v[13]);
            }
            STAMP(4);
            if (w == 0) { asm volatile("s_waitcnt vmcnt(0)" : "+v"(fl) :: "memory"); if (!__all(fl == X)) poll_eq(rws, fo, X); SIG(C_XT); } else WAITC(C_XT, 1, 1);
            STAMP(5);
            u32x4 xv[2];
#pragma unroll
            for (int j = 0; j < 2; ++j) xv[j] = __builtin_amdgcn_raw_buffer_load_b128(rws, WS_XT + b * 8192 + (128 * w + 64 * j + lane) * 16, 0, SC1);
            WAITC(C_BR, 4, 0);
            if (w == 0 && lane < 60) st<u32x4>(L_R2 + (lane >= 30 ? 4576 - 480 : 0) + lane * 16, (u32x4){0u, 0u, 0u, 0u});
            {
                const u32 xo = L_R2 + ((lane >> 2) & 1) * 4576 + 480 + (16 * w + (lane >> 3)) * 64 + (lane & 3) * 16;
                st<u32x4>(xo, xv[0]); st<u32x4>(xo + 512, xv[1]);
            }
            SIG(C_BS);
            const u32 ka = L_R1 + ch * 8320 + ((l15 & 1) ? 0 : 4160 + 2) + (2031 + 8 * G - l15) * 2 - nh * 256 - 192 - 512;
            const u32 xa = L_R2 + ch * 4576 + 448 + 32 * l15 + 16 * G - nh * 256 - 192;
            u32 so[4], lo[4];
#pragma unroll
            for (int j = 0; j < 4; ++j) { const int q = (j + rot) & 3; so[j] = (112 * w + 28 * q) * 896; lo[j] = L_W + (28 * q + 7 * w) * 896; }
            WAITC(C_BS, 4, 0);
            STAMP(6);
            PRIO(0);
            f32x4 acc[8];
            asm volatile(
    "s_mov_b32 s57, m0\n"
    "s_mov_b32 s60, -1\n"
    "s_mov_b32 s61, 0x00ffffff\n"
    "ds_read_b128 v[160:163], %12 offset:192\n"
    "ds_read2_b32 v[128:129], %8 offset0:176 offset1:177\n"
    "ds_read2_b32 v[130:131], %8 offset0:178 offset1:179\n"
    "ds_read_b128 v[164:167], %12 offset:704\n"
    "ds_read2_b32 v[132:133], %8 offset0:48 offset1:49\n"
    "ds_read2_b32 v[134:135], %8 offset0:50 offset1:51\n"
    "ds_read_b128 v[168:171], %12 offset:1216\n"
    "ds_read2_b32 v[136:137], %9 offset0:176 offset1:177\n"
    "ds_read2_b32 v[138:139], %9 offset0:178 offset1:179\n"
    "ds_read_b128 v[172:175], %12 offset:1728\n"
    "ds_read2_b32 v[140:141], %9 offset0:48 offset1:49\n"
    "ds_read2_b32 v[142:143], %9 offset0:50 offset1:51\n"
    "ds_read_b128 v[176:179], %12 offset:2240\n"
    "ds_read2_b32 v[144:145], %10 offset0:176 offset1:177\n"
    "ds_read2_b32 v[146:147], %10 offset0:178 offset1:179\n"
    "ds_read_b128 v[180:183], %12 offset:2752\n"
    "ds_read2_b32 v[148:149], %10 offset0:48 offset1:49\n"
    "ds_read2_b32 v[150:151], %10 offset0:50 offset1:51\n"
    "ds_read_b128 v[184:187], %12 offset:3264\n"
    "ds_read2_b32 v[152:153], %11 offset0:176 offset1:177\n"
    "ds_read2_b32 v[154:155], %11 offset0:178 offset1:179\n"
    "ds_read_b128 v[188:191], %12 offset:3776\n"
    "ds_read2_b32 v[156:157], %11 offset0:48 offset1:49\n"
    "ds_read2_b32 v[158:159], %11 offset0:50 offset1:51\n"
    "s_waitcnt lgkmcnt(15)\n"
    "v_mfma_f32_16x16x32_bf16 %0, v[128:131], v[160:163], 0\n"
    "v_mfma_f32_16x16x32_bf16 %1, v[128:131], v[164:167], 0\n"
    "v_mfma_f32_16x16x32_bf16 %1, v[132:135], v[160:163], %1\n"
    "v_mfma_f32_16x16x32_bf16 %2, v[132:135], v[164:167], 0\n"
    "v_mfma_f32_16x16x32_bf16 %2, v[128:131], v[168:171], %2\n"
    "v_mfma_f32_16x16x32_bf16 %2, v[136:139], v[160:163], %2\n"
    "v_mfma_f32_16x16x32_bf16 %3, v[132:135], v[168:171], 0\n"
    "v_mfma_f32_16x16x32_bf16 %3, v[136:139], v[164:167], %3\n"
    "v_mfma_f32_16x16x32_bf16 %4, v[136:139], v[168:171], 0\n"
    "ds_read_b128 v[224:227], %12 offset:128\n"
    "s_waitcnt lgkmcnt(15)\n"
    "v_mfma_f32_16x16x32_bf16 %3, v[128:131], v[172:175], %3\n"
    "ds_read2_b32 v[192:193], %8 offset0:160 offset1:161\n"
    "s_waitcnt lgkmcnt(14)\n"
    "v_mfma_f32_16x16x32_bf16 %3, v[140:143], v[160:163], %3\n"
    "ds_read2_b32 v[194:195], %8 offset0:162 offset1:163\n"
    "v_mfma_f32_16x16x32_bf16 %4, v[132:135], v[172:175], %4\n"
    "ds_read_b128 v[228:231], %12 offset:640\n"
    "v_mfma_f32_16x16x32_bf16 %4, v[140:143], v[164:167], %4\n"
    "ds_read2_b32 v[196:197], %8 offset0:32 offset1:33\n"
    "v_mfma_f32_16x16x32_bf16 %5, v[136:139], v[172:175], 0\n"
    "ds_read2_b32 v[198:199], %8 offset0:34 offset1:35\n"
    "v_mfma_f32_16x16x32_bf16 %5, v[140:143], v[168:171], %5\n"
    "ds_read_b128 v[232:235], %12 offset:1152\n"
    "v_mfma_f32_16x16x32_bf16 %6, v[140:143], v[172:175], 0\n"
    "ds_read2_b32 v[200:201], %9 offset0:160 offset1:161\n"
    "s_waitcnt lgkmcnt(15)\n"
    "v_mfma_f32_16x16x32_bf16 %4, v[128:131], v[176:179], %4\n"
    "ds_read2_b32 v[202:203], %9 offset0:162 offset1:163\n"
    "v_mfma_f32_16x16x32_bf16 %4, v[144:147], v[160:163], %4\n"
    "ds_read_b128 v[236:239], %12 offset:1664\n"
    "v_mfma_f32_16x16x32_bf16 %5, v[132:135], v[176:179], %5\n"
    "ds_read2_b32 v[204:205], %9 offset0:32 offset1:33\n"
    "v_mfma_f32_16x16x32_bf16 %5, v[144:147], v[164:167], %5\n"
    "ds_read2_b32 v[206:207], %9 offset0:34 offset1:35\n"
    "v_mfma_f32_16x16x32_bf16 %6, v[136:139], v[176:179], %6\n"
    "ds_read_b128 v[240:243], %12 offset:2176\n"
    "v_mfma_f32_16x16x32_bf16 %6, v[144:147], v[168:171], %6\n"
    "ds_read2_b32 v[208:209], %10 offset0:160 offset1:161\n"
    "v_mfma_f32_16x16x32_bf16 %7, v[140:143], v[176:179], 0\n"
    "ds_read2_b32 v[210:211], %10 offset0:162 offset1:163\n"
    "v_mfma_f32_16x16x32_bf16 %7, v[144:147], v[172:175], %7\n"
    "ds_read_b128 v[244:247], %12 offset:2688\n"
    "v_mfma_f32_16x16x32_bf16 %5, v[128:131], v[180:183], %5\n"
    "ds_read2_b32 v[212:213], %10 offset0:32 offset1:33\n"
    "s_waitcnt lgkmcnt(15)\n"
    "v_mfma_f32_16x16x32_bf16 %5, v[148:151], v[160:163], %5\n"
    "ds_read2_b32 v[214:215], %10 offset0:34 offset1:35\n"
    "v_mfma_f32_16x16x32_bf16 %6, v[132:135], v[180:183], %6\n"
    "ds_read_b128 v[248:251], %12 offset:3200\n"
    "v_mfma_f32_16x16x32_bf16 %6, v[148:151], v[164:167], %6\n"
    "ds_read2_b32 v[216:217], %11 offset0:160 offset1:161\n"
    "v_mfma_f32_16x16x32_bf16 %7, v[136:139], v[180:183], %7\n"
    "ds_read2_b32 v[218:219], %11 offset0:162 offset1:163\n"
    "v_mfma_f32_16x16x32_bf16 %7, v[148:151], v[168:171], %7\n"
    "ds_read_b128 v[252:255], %12 offset:3712\n"
    "v_mfma_f32_16x16x32_bf16 %6, v[128:131], v[184:187], %6\n"
    "ds_read2_b32 v[220:221], %11 offset0:32 offset1:33\n"
    "v_mfma_f32_16x16x32_bf16 %6, v[152:155], v[160:163], %6\n"
    "ds_read2_b32 v[222:223], %11 offset0:34 offset1:35\n"
    "v_mfma_f32_16x16x32_bf16 %7, v[132:135], v[184:187], %7\n"
    "v_mfma_f32_16x16x32_bf16 %7, v[152:155], v[164:167], %7\n"
    "v_mfma_f32_16x16x32_bf16 %7, v[128:131], v[188:191], %7\n"
    "v_mfma_f32_16x16x32_bf16 %7, v[156:159], v[160:163], %7\n"
    "s_mov_b32 s58, 0x100000\n"
    "Lqr_%=:\n"
    "ds_read_b32 v62, %23 offset:24\n"
    "s_waitcnt lgkmcnt(0)\n"
    "v_readfirstlane_b32 s59, v62\n"
    "s_cmp_ge_u32 s59, 4\n"
    "s_cbranch_scc1 Lqrx_%=\n"
    "s_sleep 1\n"
    "s_sub_u32 s58, s58, 1\n"
    "s_cbranch_scc0 Lqr_%=\n"
    "Lqrx_%=:\n"
    "s_mov_b32 s56, %15\n"
    "s_mov_b32 m0, %19\n"
    "s_mov_b64 exec, s[60:61]\n"
    "buffer_load_dwordx4 %14, %13, s56 offen lds\n"
    "s_add_u32 m0, m0, 896\n"
    "s_add_u32 s56, s56, 3584\n"
    "buffer_load_dwordx4 %14, %13, s56 offen lds\n"
    "s_add_u32 m0, m0, 896\n"
    "s_add_u32 s56, s56, 3584\n"
    "buffer_load_dwordx4 %14, %13, s56 offen lds\n"
    "s_add_u32 m0, m0, 896\n"
    "s_add_u32 s56, s56, 3584\n"
    "buffer_load_dwordx4 %14, %13, s56 offen lds\n"
    "s_add_u32 m0, m0, 896\n"
    "s_add_u32 s56, s56, 3584\n"
    "buffer_load_dwordx4 %14, %13, s56 offen lds\n"
    "s_add_u32 m0, m0, 896\n"
    "s_add_u32 s56, s56, 3584\n"
    "buffer_load_dwordx4 %14, %13, s56 offen lds\n"
    "s_add_u32 m0, m0, 896\n"
    "s_add_u32 s56, s56, 3584\n"
    "buffer_load_dwordx4 %14, %13, s56 offen lds\n"
    "s_mov_b64 exec, -1\n"
    "v_mfma_f32_16x16x32_bf16 %0, v[192:195], v[224:227], %0\n"
    "v_mfma_f32_16x16x32_bf16 %1, v[192:195], v[228:231], %1\n"
    "v_mfma_f32_16x16x32_bf16 %1, v[196:199], v[224:227], %1\n"
    "v_mfma_f32_16x16x32_bf16 %2, v[196:199], v[228:231], %2\n"
    "v_mfma_f32_16x16x32_bf16 %2, v[192:195], v[232:235], %2\n"
    "v_mfma_f32_16x16x32_bf16 %2, v[200:203], v[224:227], %2\n"
    "v_mfma_f32_16x16x32_bf16 %3, v[196:199], v[232:235], %3\n"
    "v_mfma_f32_16x16x32_bf16 %3, v[200:203], v[228:231], %3\n"
    "v_mfma_f32_16x16x32_bf16 %4, v[200:203], v[232:235], %4\n"
    "ds_read_b128 v[160:163], %12 offset:64\n"
    "v_mfma_f32_16x16x32_bf16 %3, v[192:195], v[236:239], %3\n"
    "ds_read2_b32 v[128:129], %8 offset0:144 offset1:145\n"
    "v_mfma_f32_16x16x32_bf16 %3, v[204:207], v[224:227], %3\n"
    "ds_read2_b32 v[130:131], %8 offset0:146 offset1:147\n"
    "v_mfma_f32_16x16x32_bf16 %4, v[196:199], v[236:239], %4\n"
    "ds_read_b128 v[164:167], %12 offset:576\n"
    "v_mfma_f32_16x16x32_bf16 %4, v[204:207], v[228:231], %4\n"
    "ds_read2_b32 v[132:133], %8 offset0:16 offset1:17\n"
    "v_mfma_f32_16x16x32_bf16 %5, v[200:203], v[236:239], %5\n"
    "ds_read2_b32 v[134:135], %8 offset0:18 offset1:19\n"
    "v_mfma_f32_16x16x32_bf16 %5, v[204:207], v[232:235], %5\n"
    "ds_read_b128 v[168:171], %12 offset:1088\n"
    "v_mfma_f32_16x16x32_bf16 %6, v[204:207], v[236:239], %6\n"
    "ds_read2_b32 v[136:137], %9 offset0:144 offset1:145\n"
    "v_mfma_f32_16x16x32_bf16 %4, v[192:195], v[240:243], %4\n"
    "ds_read2_b32 v[138:139], %9 offset0:146 offset1:147\n"
    "v_mfma_f32_16x16x32_bf16 %4, v[208:211], v[224:227], %4\n"
    "ds_read_b128 v[172:175], %12 offset:1600\n"
    "v_mfma_f32_16x16x32_bf16 %5, v[196:199], v[240:243], %5\n"
    "ds_read2_b32 v[140:141], %9 offset0:16 offset1:17\n"
    "v_mfma_f32_16x16x32_bf16 %5, v[208:211], v[228:231], %5\n"
    "ds_read2_b32 v[142:143], %9 offset0:18 offset1:19\n"
    "v_mfma_f32_16x16x32_bf16 %6, v[200:203], v[240:243], %6\n"
    "ds_read_b128 v[176:179], %12 offset:2112\n"
    "v_mfma_f32_16x16x32_bf16 %6, v[208:211], v[232:235], %6\n"
    "ds_read2_b32 v[144:145], %10 offset0:144 offset1:145\n"
    "v_mfma_f32_16x16x32_bf16 %7, v[204:207], v[240:243], %7\n"
    "ds_read2_b32 v[146:147], %10 offset0:146 offset1:147\n"
    "v_mfma_f32_16x16x32_bf16 %7, v[208:211], v[236:239], %7\n"
    "ds_read_b128 v[180:183], %12 offset:2624\n"
    "v_mfma_f32_16x16x32_bf16 %5, v[192:195], v[244:247], %5\n"
    "ds_read2_b32 v[148:149], %10 offset0:16 offset1:17\n"
    "v_mfma_f32_16x16x32_bf16 %5, v[212:215], v[224:227], %5\n"
    "ds_read2_b32 v[150:151], %10 offset0:18 offset1:19\n"
    "v_mfma_f32_16x16x32_bf16 %6, v[196:199], v[244:247], %6\n"
    "ds_read_b128 v[184:187], %12 offset:3136\n"
    "v_mfma_f32_16x16x32_bf16 %6, v[212:215], v[228:231], %6\n"
    "ds_read2_b32 v[152:153], %11 offset0:144 offset1:145\n"
    "v_mfma_f32_16x16x32_bf16 %7, v[200:203], v[244:247], %7\n"
    "ds_read2_b32 v[154:155], %11 offset0:146 offset1:147\n"
    "v_mfma_f32_16x16x32_bf16 %7, v[212:215], v[232:235], %7\n"
    "ds_read_b128 v[188:191], %12 offset:3648\n"
    "v_mfma_f32_16x16x32_bf16 %6, v[192:195], v[248:251], %6\n"
    "ds_read2_b32 v[156:157], %11 offset0:16 offset1:17\n"
    "v_mfma_f32_16x16x32_bf16 %6, v[216:219], v[224:227], %6\n"
    "ds_read2_b32 v[158:159], %11 offset0:18 offset1:19\n"
    "v_mfma_f32_16x16x32_bf16 %7, v[196:199], v[248:251], %7\n"
    "v_mfma_f32_16x16x32_bf16 %7, v[216:219], v[228:231], %7\n"
    "v_mfma_f32_16x16x32_bf16 %7, v[192:195], v[252:255], %7\n"
    "v_mfma_f32_16x16x32_bf16 %7, v[220:223], v[224:227], %7\n"
    "s_mov_b32 s56, %16\n"
    "s_mov_b32 m0, %20\n"
    "s_mov_b64 exec, s[60:61]\n"
    "buffer_load_dwordx4 %14, %13, s56 offen lds\n"
    "s_add_u32 m0, m0, 896\n"
    "s_add_u32 s56, s56, 3584\n"
    "buffer_load_dwordx4 %14, %13, s56 offen lds\n"
    "s_add_u32 m0, m0, 896\n"
    "s_add_u32 s56, s56, 3584\n"
    "buffer_load_dwordx4 %14, %13, s56 offen lds\n"
    "s_add_u32 m0, m0, 896\n"
    "s_add_u32 s56, s56, 3584\n"
    "buffer_load_dwordx4 %14, %13, s56 offen lds\n"
    "s_add_u32 m0, m0, 896\n"
    "s_add_u32 s56, s56, 3584\n"
    "buffer_load_dwordx4 %14, %13, s56 offen lds\n"
    "s_add_u32 m0, m0, 896\n"
    "s_add_u32 s56, s56, 3584\n"
    "buffer_load_dwordx4 %14, %13, s56 offen lds\n"
    "s_add_u32 m0, m0, 896\n"
    "s_add_u32 s56, s56, 3584\n"
    "buffer_load_dwordx4 %14, %13, s56 offen lds\n"
    "s_mov_b64 exec, -1\n"
    "s_waitcnt lgkmcnt(15)\n"
    "v_mfma_f32_16x16x32_bf16 %0, v[128:131], v[160:163], %0\n"
    "v_mfma_f32_16x16x32_bf16 %1, v[128:131], v[164:167], %1\n"
    "v_mfma_f32_16x16x32_bf16 %1, v[132:135], v[160:163], %1\n"
    "v_mfma_f32_16x16x32_bf16 %2, v[132:135], v[164:167], %2\n"
    "v_mfma_f32_16x16x32_bf16 %2, v[128:131], v[168:171], %2\n"
    "v_mfma_f32_16x16x32_bf16 %2, v[136:139], v[160:163], %2\n"
    "v_mfma_f32_16x16x32_bf16 %3, v[132:135], v[168:171], %3\n"
    "v_mfma_f32_16x16x32_bf16 %3, v[136:139], v[164:167], %3\n"
    "v_mfma_f32_16x16x32_bf16 %4, v[136:139], v[168:171], %4\n"
    "ds_read_b128 v[224:227], %12 offset:0\n"
    "s_waitcnt lgkmcnt(15)\n"
    "v_mfma_f32_16x16x32_bf16 %3, v[128:131], v[172:175], %3\n"
    "ds_read2_b32 v[192:193], %8 offset0:128 offset1:129\n"
    "s_waitcnt lgkmcnt(14)\n"
    "v_mfma_f32_16x16x32_bf16 %3, v[140:143], v[160:163], %3\n"
    "ds_read2_b32 v[194:195], %8 offset0:130 offset1:131\n"
    "v_mfma_f32_16x16x32_bf16 %4, v[132:135], v[172:175], %4\n"
    "ds_read_b128 v[228:231], %12 offset:512\n"
    "v_mfma_f32_16x16x32_bf16 %4, v[140:143], v[164:167], %4\n"
    "ds_read2_b32 v[196:197], %8 offset0:0 offset1:1\n"
    "v_mfma_f32_16x16x32_bf16 %5, v[136:139], v[172:175], %5\n"
    "ds_read2_b32 v[198:199], %8 offset0:2 offset1:3\n"
    "v_mfma_f32_16x16x32_bf16 %5, v[140:143], v[168:171], %5\n"
    "ds_read_b128 v[232:235], %12 offset:1024\n"
    "v_mfma_f32_16x16x32_bf16 %6, v[140:143], v[172:175], %6\n"
    "ds_read2_b32 v[200:201], %9 offset0:128 offset1:129\n"
    "s_waitcnt lgkmcnt(15)\n"
    "v_mfma_f32_16x16x32_bf16 %4, v[128:131], v[176:179], %4\n"
    "ds_read2_b32 v[202:203], %9 offset0:130 offset1:131\n"
    "v_mfma_f32_16x16x32_bf16 %4, v[144:147], v[160:163], %4\n"
    "ds_read_b128 v[236:239], %12 offset:1536\n"
    "v_mfma_f32_16x16x32_bf16 %5, v[132:135], v[176:179], %5\n"
    "ds_read2_b32 v[204:205], %9 offset0:0 offset1:1\n"
    "v_mfma_f32_16x16x32_bf16 %5, v[144:147], v[164:167], %5\n"
    "ds_read2_b32 v[206:207], %9 offset0:2 offset1:3\n"
    "v_mfma_f32_16x16x32_bf16 %6, v[136:139], v[176:179], %6\n"
    "ds_read_b128 v[240:243], %12 offset:2048\n"
    "v_mfma_f32_16x16x32_bf16 %6, v[144:147], v[168:171], %6\n"
    "ds_read2_b32 v[208:209], %10 offset0:128 offset1:129\n"
    "v_mfma_f32_16x16x32_bf16 %7, v[140:143], v[176:179], %7\n"
    "ds_read2_b32 v[210:211], %10 offset0:130 offset1:131\n"
    "v_mfma_f32_16x16x32_bf16 %7, v[144:147], v[172:175], %7\n"
    "ds_read_b128 v[244:247], %12 offset:2560\n"
    "v_mfma_f32_16x16x32_bf16 %5, v[128:131], v[180:183], %5\n"
    "ds_read2_b32 v[212:213], %10 offset0:0 offset1:1\n"
    "s_waitcnt lgkmcnt(15)\n"
    "v_mfma_f32_16x16x32_bf16 %5, v[148:151], v[160:163], %5\n"
    "ds_read2_b32 v[214:215], %10 offset0:2 offset1:3\n"
    "v_mfma_f32_16x16x32_bf16 %6, v[132:135], v[180:183], %6\n"
    "ds_read_b128 v[248:251], %12 offset:3072\n"
    "v_mfma_f32_16x16x32_bf16 %6, v[148:151], v[164:167], %6\n"
    "ds_read2_b32 v[216:217], %11 offset0:128 offset1:129\n"
    "v_mfma_f32_16x16x32_bf16 %7, v[136:139], v[180:183], %7\n"
    "ds_read2_b32 v[218:219], %11 offset0:130 offset1:131\n"
    "v_mfma_f32_16x16x32_bf16 %7, v[148:151], v[168:171], %7\n"
    "ds_read_b128 v[252:255], %12 offset:3584\n"
    "v_mfma_f32_16x16x32_bf16 %6, v[128:131], v[184:187], %6\n"
    "ds_read2_b32 v[220:221], %11 offset0:0 offset1:1\n"
    "v_mfma_f32_16x16x32_bf16 %6, v[152:155], v[160:163], %6\n"
    "ds_read2_b32 v[222:223], %11 offset0:2 offset1:3\n"
    "v_mfma_f32_16x16x32_bf16 %7, v[132:135], v[184:187], %7\n"
    "v_mfma_f32_16x16x32_bf16 %7, v[152:155], v[164:167], %7\n"
    "v_mfma_f32_16x16x32_bf16 %7, v[128:131], v[188:191], %7\n"
    "v_mfma_f32_16x16x32_bf16 %7, v[156:159], v[160:163], %7\n"
    "s_mov_b32 s56, %17\n"
    "s_mov_b32 m0, %21\n"
    "s_mov_b64 exec, s[60:61]\n"
    "buffer_load_dwordx4 %14, %13, s56 offen lds\n"
    "s_add_u32 m0, m0, 896\n"
    "s_add_u32 s56, s56, 3584\n"
    "buffer_load_dwordx4 %14, %13, s56 offen lds\n"
    "s_add_u32 m0, m0, 896\n"
    "s_add_u32 s56, s56, 3584\n"
    "buffer_load_dwordx4 %14, %13, s56 offen lds\n"
    "s_add_u32 m0, m0, 896\n"
    "s_add_u32 s56, s56, 3584\n"
    "buffer_load_dwordx4 %14, %13, s56 offen lds\n"
    "s_add_u32 m0, m0, 896\n"
    "s_add_u32 s56, s56, 3584\n"
    "buffer_load_dwordx4 %14, %13, s56 offen lds\n"
    "s_add_u32 m0, m0, 896\n"
    "s_add_u32 s56, s56, 3584\n"
    "buffer_load_dwordx4 %14, %13, s56 offen lds\n"
    "s_add_u32 m0, m0, 896\n"
    "s_add_u32 s56, s56, 3584\n"
    "buffer_load_dwordx4 %14, %13, s56 offen lds\n"
    "s_mov_b64 exec, -1\n"
    "s_waitcnt lgkmcnt(15)\n"
    "v_mfma_f32_16x16x32_bf16 %0, v[192:195], v[224:227], %0\n"
    "v_mfma_f32_16x16x32_bf16 %1, v[192:195], v[228:231], %1\n"
    "v_mfma_f32_16x16x32_bf16 %1, v[196:199], v[224:227], %1\n"
    "v_mfma_f32_16x16x32_bf16 %2, v[196:199], v[228:231], %2\n"
    "v_mfma_f32_16x16x32_bf16 %2, v[192:195], v[232:235], %2\n"
    "v_mfma_f32_16x16x32_bf16 %2, v[200:203], v[224:227], %2\n"
    "v_mfma_f32_16x16x32_bf16 %3, v[196:199], v[232:235], %3\n"
    "v_mfma_f32_16x16x32_bf16 %3, v[200:203], v[228:231], %3\n"
    "v_mfma_f32_16x16x32_bf16 %4, v[200:203], v[232:235], %4\n"
    "s_waitcnt lgkmcnt(14)\n"
    "v_mfma_f32_16x16x32_bf16 %3, v[192:195], v[236:239], %3\n"
    "s_waitcnt lgkmcnt(12)\n"
    "v_mfma_f32_16x16x32_bf16 %3, v[204:207], v[224:227], %3\n"
    "v_mfma_f32_16x16x32_bf16 %4, v[196:199], v[236:239], %4\n"
    "v_mfma_f32_16x16x32_bf16 %4, v[204:207], v[228:231], %4\n"
    "v_mfma_f32_16x16x32_bf16 %5, v[200:203], v[236:239], %5\n"
    "v_mfma_f32_16x16x32_bf16 %5, v[204:207], v[232:235], %5\n"
    "v_mfma_f32_16x16x32_bf16 %6, v[204:207], v[236:239], %6\n"
    "s_waitcnt lgkmcnt(11)\n"
    "v_mfma_f32_16x16x32_bf16 %4, v[192:195], v[240:243], %4\n"
    "s_waitcnt lgkmcnt(9)\n"
    "v_mfma_f32_16x16x32_bf16 %4, v[208:211], v[224:227], %4\n"
    "v_mfma_f32_16x16x32_bf16 %5, v[196:199], v[240:243], %5\n"
    "v_mfma_f32_16x16x32_bf16 %5, v[208:211], v[228:231], %5\n"
    "v_mfma_f32_16x16x32_bf16 %6, v[200:203], v[240:243], %6\n"
    "v_mfma_f32_16x16x32_bf16 %6, v[208:211], v[232:235], %6\n"
    "v_mfma_f32_16x16x32_bf16 %7, v[204:207], v[240:243], %7\n"
    "v_mfma_f32_16x16x32_bf16 %7, v[208:211], v[236:239], %7\n"
    "s_waitcnt lgkmcnt(8)\n"
    "v_mfma_f32_16x16x32_bf16 %5, v[192:195], v[244:247], %5\n"
    "s_waitcnt lgkmcnt(6)\n"
    "v_mfma_f32_16x16x32_bf16 %5, v[212:215], v[224:227], %5\n"
    "v_mfma_f32_16x16x32_bf16 %6, v[196:199], v[244:247], %6\n"
    "v_mfma_f32_16x16x32_bf16 %6, v[212:215], v[228:231], %6\n"
    "v_mfma_f32_16x16x32_bf16 %7, v[200:203], v[244:247], %7\n"
    "v_mfma_f32_16x16x32_bf16 %7, v[212:215], v[232:235], %7\n"
    "s_waitcnt lgkmcnt(5)\n"
    "v_mfma_f32_16x16x32_bf16 %6, v[192:195], v[248:251], %6\n"
    "s_waitcnt lgkmcnt(3)\n"
    "v_mfma_f32_16x16x32_bf16 %6, v[216:219], v[224:227], %6\n"
    "v_mfma_f32_16x16x32_bf16 %7, v[196:199], v[248:251], %7\n"
    "v_mfma_f32_16x16x32_bf16 %7, v[216:219], v[228:231], %7\n"
    "s_waitcnt lgkmcnt(2)\n"
    "v_mfma_f32_16x16x32_bf16 %7, v[192:195], v[252:255], %7\n"
    "s_waitcnt lgkmcnt(0)\n"
    "v_mfma_f32_16x16x32_bf16 %7, v[220:223], v[224:227], %7\n"
    "s_mov_b32 s56, %18\n"
    "s_mov_b32 m0, %22\n"
    "s_mov_b64 exec, s[60:61]\n"
    "buffer_load_dwordx4 %14, %13, s56 offen lds\n"
    "s_add_u32 m0, m0, 896\n"
    "s_add_u32 s56, s56, 3584\n"
    "buffer_load_dwordx4 %14, %13, s56 offen lds\n"
    "s_add_u32 m0, m0, 896\n"
    "s_add_u32 s56, s56, 3584\n"
    "buffer_load_dwordx4 %14, %13, s56 offen lds\n"
    "s_add_u32 m0, m0, 896\n"
    "s_add_u32 s56, s56, 3584\n"
    "buffer_load_dwordx4 %14, %13, s56 offen lds\n"
    "s_add_u32 m0, m0, 896\n"
    "s_add_u32 s56, s56, 3584\n"
    "buffer_load_dwordx4 %14, %13, s56 offen lds\n"
    "s_add_u32 m0, m0, 896\n"
    "s_add_u32 s56, s56, 3584\n"
    "buffer_load_dwordx4 %14, %13, s56 offen lds\n"
    "s_add_u32 m0, m0, 896\n"
    "s_add_u32 s56, s56, 3584\n"
    "buffer_load_dwordx4 %14, %13, s56 offen lds\n"
    "s_mov_b64 exec, -1\n"
    "s_mov_b32 m0, s57\n"
    "s_nop 7\n"

                : "=&v"(acc[0]), "=&v"(acc[1]), "=&v"(acc[2]), "=&v"(acc[3]), "=&v"(acc[4]), "=&v"(acc[5]), "=&v"(acc[6]), "=&v"(acc[7])
                : "v"(ka), "v"(ka - 1024), "v"(ka - 2048), "v"(ka - 3072), "v"(xa), "s"(ro), "v"(vrow),
                  "s"(so[0]), "s"(so[1]), "s"(so[2]), "s"(so[3]), "s"(lo[0]), "s"(lo[1]), "s"(lo[2]), "s"(lo[3]), "v"(vM)
                : "memory", "scc", "v62", "v63", "v128", "v129", "v130", "v131", "v132", "v133", "v134", "v135", "v136", "v137", "v138", "v139", "v140", "v141", "v142", "v143", "v144", "v145", "v146", "v147", "v148", "v149", "v150", "v151", "v152", "v153", "v154", "v155", "v156", "v157", "v158", "v159", "v160", "v161", "v162", "v163", "v164", "v165", "v166", "v167", "v168", "v169", "v170", "v171", "v172", "v173", "v174", "v175", "v176", "v177", "v178", "v179", "v180", "v181", "v182", "v183", "v184", "v185", "v186", "v187", "v188", "v189", "v190", "v191", "v192", "v193", "v194", "v195", "v196", "v197", "v198", "v199", "v200", "v201", "v202", "v203", "v204", "v205", "v206", "v207", "v208", "v209", "v210", "v211", "v212", "v213", "v214", "v215", "v216", "v217", "v218", "v219", "v220", "v221", "v222", "v223", "v224", "v225", "v226", "v227", "v228", "v229", "v230", "v231", "v232", "v233", "v234", "v235", "v236", "v237", "v238", "v239", "v240", "v241", "v242", "v243", "v244", "v245", "v246", "v247", "v248", "v249", "v250", "v251", "v252", "v253", "v254", "v255", "s56", "s57", "s58", "s59", "s60", "s61");
            PRIO(3);
            STAMP(7);
            auto exa = [&](int T) { return (T < 28 ? L_X + T * 1024 : L_R2 + 9216 + (T - 28) * 1024) + lane * 16; };
#pragma unroll
            for (int J = 0; J < 8; ++J) st<f32x4>(exa(w * 8 + J), acc[J]);
            SIG(C_CX);
            WAITC(C_CX, 4, 1);
            f32x4 yy[2][4];
#pragma unroll
            for (int e = 0; e < 2; ++e)
#pragma unroll
                for (int k = 0; k < 4; ++k) yy[e][k] = ld<f32x4>(exa(8 * k + 2 * w + e));
            SIG(C_CR);
#pragma unroll
            for (int e = 0; e < 2; ++e) {
                const f32x4 y0 = yy[e][0] + yy[e][1], y1 = yy[e][2] + yy[e][3];
                __builtin_amdgcn_raw_buffer_store_b128((u32x4){nopoison(pk(y0[0], y1[0])), nopoison(pk(y0[1], y1[1])), nopoison(pk(y0[2], y1[2])), nopoison(pk(y0[3], y1[3]))},
                                                       rws, WS_BT + b * 8192 + 64 * l15 + 16 * G + e * 1024, w * 2048, SC1);
            }
        } else {
            WAITC(C_QR, 4, 4);
            if (lane < 56) {
#pragma unroll 1
                for (int j = 0; j < 4; ++j) { const int q = (j + rot) & 3; dma_run(ro, vrow, (112 * w + 28 * q) * 896, 3584, ~0u, 0, L_W + (28 * q + 7 * w) * 896, 896, 0, 7); }
            }
        }
        STAMP(8);
        vm0();
        SIG(C_WO);
        WAITC(C_WO, 4, 1);
        u32x4 Wf[2][14];
        {
            const u32 wa = L_W + (4 * G + (l15 >> 2)) * 224 + w * 64 + (lane & 3) * 8;
#pragma unroll
            for (int s = 0; s < 14; ++s)
#pragma unroll
                for (int n = 0; n < 2; ++n) Wf[n][s] = cat(ldtr(wa + s * 7168 + n * 32), ldtr(wa + s * 7168 + n * 32 + 3584));
        }
        SIG(C_WR);
        float gg[2], og[2];
#pragma unroll
        for (int P = 0; P < 2; ++P) { const u32 ga = L_G + (16 * P + l15) * 4;
            const float s = rbf((ld<float>(ga) + ld<float>(ga + 128)) + (ld<float>(ga + 256) + ld<float>(ga + 384)));
            gg[P] = rbf(__builtin_amdgcn_rcpf(1.0f + __builtin_amdgcn_exp2f(s * -1.4426950408889634f))); og[P] = rbf(1.0f - gg[P]); }
        if (has) WAITC(C_CR, 4, 1);
        STAMP(9);
        {
            const int dq = 2 * w + h;
            const u32 off = dq * 256 + (((l31 >> 4) ^ ((m + dq) & 1)) * 128) + l15 * 8;
            const u32 po = L_W + w * 1024 + lane * 16, pl = L_W + 3072 + (32 + l31) * 16, go = WS_OX + (g * 4 + m) * 7168 + off, so = L_X + 7 * m * 256 + off;
            u32 published = 0;
            for (int pass = 0; pass < 2; ++pass) {
#pragma unroll
                for (int q = 0; q < 4; ++q) {
                    if (q < 3) WAITC(C_OP0 + q, 4 * (pass + 1), 2); else WAITC(C_OP0 + q, 4 * (pass + 1), 0);
                    if (q == 3) STAMP(10);
                    f32x4 o4[4]; float l4[4];
#pragma unroll
                    for (int a = 0; a < 4; ++a) { l4[a] = ld<float>(pl + q * 16384 + a * 4096); o4[a] = ld<f32x4>(po + q * 16384 + a * 4096); }
                    SB();
                    const float l = (l4[0] + l4[1]) + (l4[2] + l4[3]);
                    const f32x4 o = ((o4[0] + o4[1]) + (o4[2] + o4[3])) * __builtin_amdgcn_rcpf(l);
                    const bool ok = __all(l > 0x1p-64f && l < 0x1p64f);
                    if ((ok || pass) && !((published >> q) & 1)) {
                        published |= 1u << q;
                        const u32x2 v = {nopoison(pk(o[0], o[1])), nopoison(pk(o[2], o[3]))};
                        if (dq < 7) { __builtin_amdgcn_raw_buffer_store_b64(v, rws, go, q * 1792, SC1); st<u32x2>(so + q * 7168, v); }
                        SIG(C_OS);
                    }
                    if (q == 0 && pass == 0 && b == 0 && w == 0 && lane == 0) __builtin_amdgcn_raw_buffer_store_b32(X, rws, 0, 0, SC1);
                }
                if (pass == 0 && w == 0 && lane == 0) asm volatile("ds_write_b32 %0, %1 offset:%2" :: "v"(vM), "v"(published == 15 ? 1u : 2u), "i"(4 * C_V) : "memory");
                if (published == 15) break;
            }
        }
        STAMP(11);
        u32 bv[2][2][2];
        const u32 bo = WS_BT + (56 * m + 2 * G) * 8192 + (32 * g + l15) * 4;
        auto load_b = [&]() {
#pragma unroll
            for (int n = 0; n < 2; ++n)
#pragma unroll
                for (int P = 0; P < 2; ++P)
#pragma unroll
                    for (int e = 0; e < 2; ++e) bv[n][P][e] = __builtin_amdgcn_raw_buffer_load_b32(rws, bo + 64 * P, (8 * min(2 * w + n, 6) + e) * 8192, SC1);
        };
        load_b();
        f32x4 oa[2][2];
#pragma unroll
        for (int n = 0; n < 2; ++n) { oa[n][0] = (f32x4){0.f, 0.f, 0.f, 0.f}; oa[n][1] = oa[n][0]; }
        const u32 o0 = L_X + G * 256 + (G & 1) * 128 + l15 * 8, o1 = o0 ^ 128;
        u32x4 f0[14], f1[14];
        auto ldf = [&](int s) { f0[s] = cat(ldv<u32x2>(o0 + s * 2048), ldv<u32x2>(o0 + s * 2048 + 1024)); f1[s] = cat(ldv<u32x2>(o1 + s * 2048), ldv<u32x2>(o1 + s * 2048 + 1024)); };
        auto proj = [&](int s) {
#pragma unroll
            for (int n = 0; n < 2; ++n) { oa[n][0] = mfma16(Wf[n][s], f0[s], oa[n][0]); oa[n][1] = mfma16(Wf[n][s], f1[s], oa[n][1]); }
        };
        WAITC(C_OS, 16, 0);
        WAITC(C_G1, 3, 0);
        STAMP(12);
        ldf(0); ldf(1); ldf(2);
#pragma unroll
        for (int s = 0; s < 10; ++s) { if (s + 3 < 10) ldf(s + 3); SB(); proj(s); SB(); }
        for (u32 i = 0; i < SPIN_CAP; ++i) {
            u32 mm = 0;
#pragma unroll
            for (int n = 0; n < 2; ++n)
#pragma unroll
                for (int P = 0; P < 2; ++P) mm = max(mm, max(bv[n][P][0], bv[n][P][1]));
            if (__all(mm != ~0u)) break;
            asm volatile("" ::: "memory");
            load_b();
        }
        float tb[2][2][4];
#pragma unroll
        for (int n = 0; n < 2; ++n)
#pragma unroll
            for (int P = 0; P < 2; ++P)
#pragma unroll
                for (int e = 0; e < 2; ++e) { tb[n][P][2 * e] = og[P] * bflo(bv[n][P][e]); tb[n][P][2 * e + 1] = og[P] * bfhi(bv[n][P][e]); }
        GAS u16* const op = (GAS u16*)out + (32 * g + l15) * 448 + 112 * m + 32 * w + 4 * G;
        STAMP(13);
        WAITC(C_G2, 3, 0);
        STAMP(14);
        ldf(10); ldf(11); ldf(12); ldf(13);
        SB();
#pragma unroll
        for (int s = 10; s < 14; ++s) proj(s);
#pragma unroll
        for (int n = 0; n < 2; ++n)
            if (2 * w + n < 7) {
#pragma unroll
                for (int P = 0; P < 2; ++P)
                    *(GAS u32x2*)(op + P * 16 * 448 + n * 16) = (u32x2){pk(fmaf(gg[P], oa[n][P][0], tb[n][P][0]), fmaf(gg[P], oa[n][P][1], tb[n][P][1])),
                                                                        pk(fmaf(gg[P], oa[n][P][2], tb[n][P][2]), fmaf(gg[P], oa[n][P][3], tb[n][P][3]))};
            }
        STAMP(15);
        asm volatile("Lpf_end_b:");
    }
    STAMP_END();
}

extern "C" void kernel_launch(void* const* d_in, const int* in_sizes, int n_in, void* d_out, int out_size, void* d_ws, size_t ws_size, hipStream_t stream) {
    fused_mix_kernel<<<256, 512, 0, stream>>>((const u16*)d_in[0], (const u16*)d_in[1], (const u16*)d_in[2], (const u16*)d_in[3], (const u16*)d_in[4],
                                              (const u16*)d_in[6], (const u16*)d_in[7], (const u16*)d_in[8], (const u16*)d_in[9], (const u16*)d_in[10], (const u16*)d_in[11],
                                              (const u16*)d_in[12], (const u16*)d_in[13], (const u16*)d_in[14], (u16*)d_out, (char*)d_ws);
}
```
